```python
import torch

def _softmax(x, axis=-1):
    return torch.softmax(x, dim=axis)
def _gelu(x):
    return torch.nn.functional.gelu(x, approximate="tanh")
def _silu(x):
    return torch.nn.functional.silu(x)
def _sigmoid(x):
    return torch.sigmoid(x)
def _layernorm(x, axis=-1, eps=1e-5):
    m = x.mean(dim=axis, keepdim=True)
    v = x.var(dim=axis, keepdim=True, unbiased=False)
    return (x - m) / torch.sqrt(v + eps)
def _scatter_add(tgt, idx, src, axis=0):
    return tgt.clone().index_add_(axis, idx, src)
def _segment_sum(src, seg, n):
    out = torch.zeros((n,) + src.shape[1:], dtype=src.dtype)
    return out.index_add_(0, seg, src)
def _pad(x, pw):
    flat = tuple(v for pair in reversed(pw) for v in pair)
    return torch.nn.functional.pad(x, flat)

kernel_name = 'interleave_layers__linear_kernel_hedgehog__glu_gated_glu__prompt'

def setup_inputs():

    _w = (lambda a: (lambda t: t.to(torch.bfloat16) if t.is_floating_point() else t)(torch.as_tensor(a)))
    def _A_setup():
        B, S, D, H = 1, 2048, 448, 16
        Dh = D // H
        return {
            "x": _w((lambda *s: torch.randn(*s, dtype=torch.bfloat16) * 0.05)(B, S, D)),
            "wqkv": _w((lambda *s: torch.randn(*s, dtype=torch.bfloat16) * 0.4)(D, 3 * D)),
            "w_phi": _w((lambda *s: torch.randn(*s, dtype=torch.bfloat16) * 0.4)(H, Dh, Dh) * (Dh ** -0.5)),
            "wo": _w((lambda *s: torch.randn(*s, dtype=torch.bfloat16) * 0.05)(D, D)),
        }
    _A_in = _A_setup()

    def _B_setup():
        B, S, D, F = 1, 2048, 448, 1792
        return {
            "x": _w((lambda *s: torch.randn(*s, dtype=torch.bfloat16) * 0.05)(B, S, D)),
            "w_gate": _w((lambda *s: torch.randn(*s, dtype=torch.bfloat16) * 0.05)(D, F)),
            "w_up": _w((lambda *s: torch.randn(*s, dtype=torch.bfloat16) * 0.05)(D, F)),
            "w_down": _w((lambda *s: torch.randn(*s, dtype=torch.bfloat16) * 0.05)(F, D)),
        }
    _B_in = _B_setup()

    return {
        "A_x": _A_in["x"],
        "A_wqkv": _A_in["wqkv"],
        "A_w_phi": _A_in["w_phi"],
        "A_wo": _A_in["wo"],
        "B_x": _B_in["x"],
        "B_w_gate": _B_in["w_gate"],
        "B_w_up": _B_in["w_up"],
        "B_w_down": _B_in["w_down"],
    }

def reference(A_x, A_wqkv, A_w_phi, A_wo, B_x, B_w_gate, B_w_up, B_w_down):
    def _A_ref(x, wqkv, w_phi, wo):
        B, S, D = x.shape
        H = 16; Dh = D // H
        qkv = (lambda x, *d: x.permute(*d))((x @ wqkv).reshape(B, S, 3, H, Dh), 2, 0, 3, 1, 4)
        q, k, v = qkv[0], qkv[1], qkv[2]
        pq = torch.einsum("bhsd,hdf->bhsf", q, w_phi)
        pk = torch.einsum("bhsd,hdf->bhsf", k, w_phi)
        phi_q = torch.concatenate([_softmax(pq, axis=-1), _softmax(-pq, axis=-1)], axis=-1)
        phi_k = torch.concatenate([_softmax(pk, axis=-1), _softmax(-pk, axis=-1)], axis=-1)
        kv = torch.einsum("bhsf,bhsd->bhfd", phi_k, v)
        z  = torch.einsum("bhsf->bhf", phi_k)
        num = torch.einsum("bhqf,bhfd->bhqd", phi_q, kv)
        den = torch.einsum("bhqf,bhf->bhq", phi_q, z)[..., None] + 1e-6
        o = num / den
        return (lambda x, *d: x.permute(*d))(o, 0, 2, 1, 3).reshape(B, S, D) @ wo

    def _B_ref(x, w_gate, w_up, w_down):
        g = torch.einsum("bsd,df->bsf", x, w_gate)
        u = torch.einsum("bsd,df->bsf", x, w_up)
        h = _sigmoid(g) * u
        return torch.einsum("bsf,fd->bsd", h, w_down)

    x = A_x
    x = x + _A_ref(_layernorm(x, axis=-1), A_wqkv, A_w_phi, A_wo)
    x = x + _B_ref(_layernorm(x, axis=-1), B_w_gate, B_w_up, B_w_down)
    x = x + _A_ref(_layernorm(x, axis=-1), A_wqkv, A_w_phi, A_wo)
    x = x + _B_ref(_layernorm(x, axis=-1), B_w_gate, B_w_up, B_w_down)
    return x
```

```cpp
#include <hip/hip_runtime.h>
#include <stdint.h>

namespace {

typedef unsigned v4u __attribute__((ext_vector_type(4)));
typedef unsigned v2u __attribute__((ext_vector_type(2)));
typedef float f32x4 __attribute__((ext_vector_type(4)));
typedef float f32x2 __attribute__((ext_vector_type(2)));
typedef float f32x16 __attribute__((ext_vector_type(16)));
typedef short bf16x8 __attribute__((ext_vector_type(8)));
typedef short s16x4 __attribute__((ext_vector_type(4)));
typedef __bf16 bf16x2 __attribute__((ext_vector_type(2)));
typedef unsigned long long u64;
#define LAS __attribute__((address_space(3)))
#define GAS __attribute__((address_space(1)))
#define DI __device__ __forceinline__
typedef __amdgpu_buffer_rsrc_t rsrc_t;

#define STAMPS 0

constexpr unsigned O_CTRL = 0;
constexpr unsigned O_FLG = 4096;
constexpr unsigned O_QKVT = 32768;
constexpr unsigned O_WOT = O_QKVT + 16 * 84 * 1024;
constexpr unsigned O_GUT = O_WOT + 448 * 1024;
constexpr unsigned O_DNT = O_GUT + 3136 * 1024;
constexpr unsigned O_CSQ = O_DNT + 1568 * 1024;
constexpr unsigned O_CSG = O_CSQ + 32768;
constexpr unsigned XSZ = 128 * 56 * 256;
constexpr unsigned O_X = O_CSG + 65536;
constexpr unsigned STSZ = 64 * 14 * 256;
constexpr unsigned O_ST = O_X + 4 * XSZ;
constexpr unsigned KVPSZ = 16 * 16 * 8192;
constexpr unsigned O_KVP = O_ST + 4 * STSZ;
constexpr unsigned KVRSZ = 16 * 4096;
constexpr unsigned O_KVR = O_KVP + 2 * KVPSZ;
constexpr unsigned OFSZ = 128 * 64 * 256;
constexpr unsigned O_OF = O_KVR + 2 * KVRSZ;
constexpr unsigned HBSZ = 128 * 224 * 256;
constexpr unsigned O_HB = O_OF + 2 * OFSZ;
constexpr unsigned O_DUMX = O_HB + 2 * HBSZ;
constexpr unsigned O_DUMST = O_DUMX + XSZ;
constexpr unsigned O_STAMP = O_DUMST + STSZ;
constexpr unsigned WS_BYTES = O_STAMP + 256 * 4 * 64 * 8;

constexpr int L_SCR = 112 * 1024;
constexpr int LDS_BYTES = 160 * 1024;

constexpr unsigned POISON = 0xFFFFFFFFu;
constexpr int SPIN_CAP = 1 << 16;

struct Params {
  const unsigned short* x;
  const unsigned short* wqkv;
  const unsigned short* wphi;
  const unsigned short* wo;
  const unsigned short* wg;
  const unsigned short* wu;
  const unsigned short* wd;
  unsigned short* out;
  unsigned char* ws;
};

DI v4u ldg(rsrc_t rs, unsigned voff, unsigned soff) { return __builtin_amdgcn_raw_buffer_load_b128(rs, voff, soff, 16); }
DI v2u ldg2(rsrc_t rs, unsigned voff, unsigned soff) { return __builtin_amdgcn_raw_buffer_load_b64(rs, voff, soff, 16); }
DI void stg(v4u v, rsrc_t rs, unsigned voff, unsigned soff) { __builtin_amdgcn_raw_buffer_store_b128(v, rs, voff, soff, 16); }
DI void stg2(v2u v, rsrc_t rs, unsigned voff, unsigned soff) { __builtin_amdgcn_raw_buffer_store_b64(v, rs, voff, soff, 16); }
DI unsigned san(unsigned x) { return x < 0xFFFFFFFEu ? x : 0xFFFFFFFEu; }
DI v4u san4(v4u v) { return v4u{san(v.x), san(v.y), san(v.z), san(v.w)}; }
DI unsigned pk(float a, float b) { f32x2 v = {a, b}; return __builtin_bit_cast(unsigned, __builtin_convertvector(v, bf16x2)); }
DI float blo(unsigned u) { return __builtin_bit_cast(float, u << 16); }
DI float bhi(unsigned u) { return __builtin_bit_cast(float, u & 0xFFFF0000u); }
DI float bfr(float a) { return blo(pk(a, a)); }
DI float u2f(unsigned u) { return __builtin_bit_cast(float, u); }
DI unsigned f2u(float f) { return __builtin_bit_cast(unsigned, f); }
DI f32x16 mfma(v4u a, v4u b, f32x16 c) {
  return __builtin_amdgcn_mfma_f32_32x32x16_bf16(__builtin_bit_cast(bf16x8, a), __builtin_bit_cast(bf16x8, b), c, 0, 0, 0);
}
DI unsigned umax(unsigned a, unsigned b) { return a > b ? a : b; }
DI unsigned mx4(unsigned m, v4u v) { return umax(umax(umax(m, v.x), v.y), umax(v.z, v.w)); }
DI void bar() { asm volatile("s_waitcnt lgkmcnt(0)" ::: "memory"); __builtin_amdgcn_s_barrier(); asm volatile("" ::: "memory"); }
DI void drain() { asm volatile("s_waitcnt vmcnt(0)" ::: "memory"); }
DI void sb() { __builtin_amdgcn_sched_barrier(0); }
DI unsigned opq(unsigned v) { asm volatile("" : "+v"(v)); return v; }
DI int opqs(int v) { asm volatile("" : "+s"(v)); return v; }
DI float rcp(float x) { return __builtin_amdgcn_rcpf(x); }
DI float hsum(float x) { auto r = __builtin_amdgcn_permlane32_swap(f2u(x), f2u(x), false, false); return u2f(r[0]) + u2f(r[1]); }
DI float hmax(float x) { auto r = __builtin_amdgcn_permlane32_swap(f2u(x), f2u(x), false, false); return fmaxf(u2f(r[0]), u2f(r[1])); }
DI float hmin(float x) { auto r = __builtin_amdgcn_permlane32_swap(f2u(x), f2u(x), false, false); return fminf(u2f(r[0]), u2f(r[1])); }
DI v4u widen(unsigned ax, unsigned ay, unsigned bx, unsigned by) {
  auto r0 = __builtin_amdgcn_permlane32_swap(ax, bx, false, false);
  auto r1 = __builtin_amdgcn_permlane32_swap(ay, by, false, false);
  return v4u{r0[0], r1[0], r0[1], r1[1]};
}
DI void dma4k(const unsigned char* g, unsigned l, unsigned lane16) {
  unsigned keep;
  asm volatile("s_mov_b32 %0, m0\n\ts_mov_b32 m0, %1\n\ts_nop 0\n\t"
               "global_load_lds_dwordx4 %2, %3 sc1\n\t"
               "global_load_lds_dwordx4 %2, %3 offset:1024 sc1\n\t"
               "global_load_lds_dwordx4 %2, %3 offset:2048 sc1\n\t"
               "global_load_lds_dwordx4 %2, %3 offset:3072 sc1\n\t"
               "s_mov_b32 m0, %0"
               : "=&s"(keep) : "s"(l), "v"(lane16), "s"(g) : "memory");
}
template <int NB> DI void dma_blocks(const unsigned char* g, unsigned l, int w, unsigned lane16) {
#pragma unroll
  for (int i = 0; i < (NB + 3) / 4; ++i) {
    const int blk = w + 4 * i;
    if (4 * i + 3 < NB || blk < NB) dma4k(g + blk * 4096, l + blk * 4096, lane16);
  }
}

#define STAMP(i) do { } while (0)
#define STAMPC(i) do { } while (0)

#define VALIDATE(N, REF, RELOAD)                                              \
  do {                                                                        \
    unsigned m_ = 0;                                                          \
    _Pragma("unroll") for (int i_ = 0; i_ < (N); ++i_) m_ = mx4(m_, REF(i_)); \
    if (__builtin_expect(__any(m_ == POISON), 0)) {                           \
      _Pragma("unroll 1") for (int sp_ = 0;; ++sp_) {                         \
        if (dead || sp_ > SPIN_CAP) { dead = 1; break; }                      \
        __builtin_amdgcn_s_sleep(1);                                          \
        asm volatile("" ::: "memory");                                        \
        _Pragma("unroll") for (int i_ = 0; i_ < (N); ++i_) RELOAD(i_);        \
        unsigned n_ = 0;                                                      \
        _Pragma("unroll") for (int i_ = 0; i_ < (N); ++i_) n_ = mx4(n_, REF(i_)); \
        if (!__any(n_ == POISON)) break;                                      \
      }                                                                       \
    }                                                                         \
  } while (0)

template <int V> struct IC { static constexpr int value = V; constexpr operator int() const { return V; } };
template <int B, int E, class F> DI void sfor(F&& f) { if constexpr (B < E) { f(IC<B>{}); sfor<B + 1, E>(f); } }
struct Stats { float m, s, rs; };
DI unsigned lxpat(unsigned ln, unsigned rowb) { return ((ln >> 4) & 1) * rowb + (ln & 15) * 16 + (ln >> 5) * 256; }
DI unsigned lstpat(unsigned ln) { return (ln & 31) * 8 + (ln >> 5) * 256; }

__global__ __launch_bounds__(256, 1) void mega(Params p) {
  extern __shared__ __attribute__((aligned(16))) unsigned char smem[];
  LAS unsigned char* const L = (LAS unsigned char*)smem;
  const int tid = threadIdx.x, lane = tid & 63, w = __builtin_amdgcn_readfirstlane(tid >> 6);
  const int b = blockIdx.x, idx = b >> 3, r = (b & 7) * 2 + (idx >> 4), c = idx & 15;
  const int tok = lane & 31, hi = lane >> 5;
  const unsigned lane16 = lane * 16;
  const rsrc_t rs = __builtin_amdgcn_make_buffer_rsrc(p.ws, 0, WS_BYTES, 0x00020000);
  int dead = 0;
  STAMP(0); STAMPC(62);

  const bool act = c < 14;
  const int th = __builtin_amdgcn_readfirstlane(act ? (c >= 7 ? 1 : 0) : c - 14), cg = __builtin_amdgcn_readfirstlane(act ? (c >= 7 ? c - 7 : c) : 0);

  const u64 epoch = *(const GAS u64*)(p.ws + O_CTRL);
  const u64 tagb = (epoch ^ 0x9E3779B97F4A7C15ull) << 8;
  const unsigned tlo = __builtin_amdgcn_readfirstlane((unsigned)tagb), thi = __builtin_amdgcn_readfirstlane((unsigned)(tagb >> 32));
  const int q = r * 16 + c;
  const int T32a = 4 * r + w;
  const int T32o = 4 * r + 2 * th + (w >> 1);
#define S_XA(xb) (O_X + (xb) * XSZ + (unsigned)opqs(T32a) * (2 * 56 * 256))
#define S_STA(xb) (O_ST + (xb) * STSZ + (unsigned)opqs(T32a) * (14 * 256))
#define S_OFST(l) (O_OF + (l) * OFSZ + (unsigned)opqs(T32a) * (2 * 64 * 256) + (unsigned)opqs(c) * (4 * 256))
#define S_HBST(l) (O_HB + (l) * HBSZ + (unsigned)opqs(T32a) * (2 * 224 * 256) + (unsigned)opqs(c) * (14 * 256))
#define S_XST ((unsigned)opqs(T32o) * (2 * 56 * 256) + (unsigned)(8 * opqs(cg) + 4 * (w & 1)) * 256)
#define S_STST ((unsigned)(opqs(T32o) * 14 + 2 * opqs(cg) + (w & 1)) * 256)
#define S_KVP(l) (O_KVP + (l) * KVPSZ + (unsigned)(opqs(c) * 16 + opqs(r)) * 8192)
#define S_KVRST(l) (O_KVR + (l) * KVRSZ + (unsigned)(opqs(c) * 4096 + opqs(r) * 256))
  constexpr unsigned OOB = 0x80000000u;
  const v4u P4 = {POISON, POISON, POISON, POISON};
  const v2u P2 = {POISON, POISON};

  auto publish = [&](unsigned stage) {
    if (w == 0) { const unsigned ln = opq(lane); stg2(v2u{tlo + stage, thi}, rs, ln < 8 ? ln * 2048 : OOB, O_FLG + (unsigned)opqs(q) * 8); }
  };
  auto check = [&](unsigned stage) {
    const unsigned vo = opq(lane) * 32, so = O_FLG + (unsigned)(opqs(b) & 7) * 2048;
#pragma unroll 1
    for (int sp = 0;; ++sp) {
      asm volatile("" ::: "memory");
      const v4u a = ldg(rs, vo, so), d = ldg(rs, vo + 16, so);
      const bool ok = a.y == thi && a.w == thi && d.y == thi && d.w == thi && (a.x - tlo - stage) < 8u && (a.z - tlo - stage) < 8u &&
                      (d.x - tlo - stage) < 8u && (d.z - tlo - stage) < 8u;
      if (__all(ok)) break;
      if (dead || sp > SPIN_CAP) { dead = 1; break; }
      __builtin_amdgcn_s_sleep(4);
    }
  };
  auto poison_attn = [&](int l) {
    const unsigned ln = opq(lane), l16 = ln * 16, v64 = lxpat(ln, 64 * 256), v56 = act ? lxpat(ln, 56 * 256) : OOB;
    stg(P4, rs, l16, S_KVP(l) + w * 2048); stg(P4, rs, l16 + 1024, S_KVP(l) + w * 2048);
    stg(P4, rs, (w == 0 && ln < 16) ? l16 : OOB, S_KVRST(l));
    stg(P4, rs, v64, S_OFST(l)); stg(P4, rs, v64 + 512, S_OFST(l));
    stg(P4, rs, v56, O_X + (2 * l + 1) * XSZ + S_XST); stg(P4, rs, v56 + 512, O_X + (2 * l + 1) * XSZ + S_XST);
    stg2(P2, rs, (act && ln < 32) ? ln * 8 : OOB, O_ST + (2 * l + 1) * STSZ + S_STST);
  };
  auto poison_h = [&](int l) {
    const unsigned v224 = lxpat(opq(lane), 224 * 256), so = S_HBST(l);
#pragma unroll
    for (int pp = 0; pp < 7; ++pp) stg(P4, rs, v224 + pp * 512, so);
  };

  {
    const unsigned short* src; unsigned nbytes; int ld, estep; unsigned eb, dst, dstep, csdst = OOB;
    unsigned lvo, ldst = lane16; bool zlane = false, cut = false;
    if (w == 0 && q < 192) { const int h = q & 15, kq = (q >> 4) & 3, m = q >> 6; src = p.wqkv; nbytes = 448 * 1344 * 2; ld = 1344; estep = 16 * 1344;
      eb = 448 * m + 28 * h + 7 * kq * 16 * 1344; zlane = tok >= 28; lvo = (unsigned)(zlane ? 0 : tok) + 8 * hi * 1344;
      dstep = 3072; dst = O_QKVT + h * 84 * 1024 + (m == 0 ? 2 : m - 1) * 1024 + 7 * kq * 3072; csdst = O_CSQ + ((h * 3 + m) * 4 + kq) * 128; }
    else if (w == 0) { const int wj = q - 192, nt = wj & 1, par = (wj >> 1) & 1, h = wj >> 2;
      src = p.wo; nbytes = 448 * 448 * 2; ld = 448; estep = 64; eb = (28 * h + 16 * par) * 448 + 32 * nt; lvo = (unsigned)tok + 8 * hi * 448; cut = par && hi;
      dstep = 65536; dst = O_WOT + (h >> 2) * 16384 + ((h & 3) * 2 + par) * 2048 + nt * 1024; }
    else if (w == 1) { const int wj = q < 224 ? q : 0, nt = wj & 1, i = (wj >> 1) & 3, ww = (wj >> 3) & 3, cgg = wj >> 5;
      src = p.wd; nbytes = 1792 * 448 * 2; ld = 448; estep = 16 * 448; eb = 112 * (4 * ww + i) * 448 + 64 * cgg + 32 * nt; lvo = (unsigned)tok + 8 * hi * 448;
      dstep = 8192; dst = q < 224 ? O_DNT + (cgg * 4 + ww) * 56 * 1024 + (i * 2 + nt) * 1024 : OOB; }
    else { const int j0 = q * 2 + (w - 2), j = j0 < 448 ? j0 : 0, gk = j / 112, jj = j - gk * 112, gu = gk >> 1, kh = gk & 1;
      src = gu ? p.wu : p.wg; nbytes = 448 * 1792 * 2; ld = 1792; estep = 32 * 1792;
      eb = 16 * jj + 7 * kh * 32 * 1792; lvo = (unsigned)(tok & 15) + (8 * hi + (tok & 16)) * 1792;
      ldst = (unsigned)(tok >> 4) * 1024 + (unsigned)((tok & 15) + 32 * hi) * 16;
      dstep = 2048; dst = j0 < 448 ? O_GUT + jj * 28 * 1024 + gu * 256 + 7 * kh * 2048 : OOB; csdst = j0 < 448 ? O_CSG + (jj * 4 + gk) * 128 : OOB; }
    src = (const unsigned short*)(((u64)(unsigned)__builtin_amdgcn_readfirstlane((unsigned)((u64)src >> 32)) << 32) | (unsigned)__builtin_amdgcn_readfirstlane((unsigned)(u64)src));
    const rsrc_t rw = __builtin_amdgcn_make_buffer_rsrc((void*)src, 0, __builtin_amdgcn_readfirstlane(nbytes), 0x00020000);
    ld = __builtin_amdgcn_readfirstlane(ld); estep = __builtin_amdgcn_readfirstlane(estep); eb = __builtin_amdgcn_readfirstlane(eb);
    dst = __builtin_amdgcn_readfirstlane(dst); dstep = __builtin_amdgcn_readfirstlane(dstep); csdst = __builtin_amdgcn_readfirstlane(csdst);
    STAMP(37);
    const int tk = tid & 7, kg0 = tid >> 3;
    const int xtok = 128 * r + 8 * c + tk;
    const v4u xa = *(const GAS v4u*)(p.x + (size_t)xtok * 448 + kg0 * 8);
    const v4u xb2 = *(const GAS v4u*)(p.x + (size_t)xtok * 448 + (kg0 < 24 ? kg0 + 32 : kg0) * 8);
    unsigned e[7][8];
#pragma unroll
    for (int u = 0; u < 7; ++u)
#pragma unroll
      for (int j = 0; j < 8; ++j) e[u][j] = (unsigned short)__builtin_amdgcn_raw_buffer_load_b16(rw, lvo * 2, (eb + u * estep + j * ld) * 2, 0);
    sb();
    STAMP(31);
    poison_attn(0);
    sb();
    STAMP(32);
    {
      const unsigned xo = (unsigned)(xtok >> 4) * (56 * 256) + (unsigned)(xtok & 15) * 16 + kg0 * 256;
      stg(xa, rs, xo, O_X);
      stg(xb2, rs, kg0 < 24 ? xo : OOB, O_X + 32 * 256);
#pragma unroll
      for (int z = 0; z < 2; ++z) {
        const v4u v = z ? xb2 : xa;
        float f[8] = {blo(v.x), bhi(v.x), blo(v.y), bhi(v.y), blo(v.z), bhi(v.z), blo(v.w), bhi(v.w)};
        float s1 = ((f[0] + f[1]) + (f[2] + f[3])) + ((f[4] + f[5]) + (f[6] + f[7]));
        s1 += __shfl_xor(s1, 8); s1 += __shfl_xor(s1, 16);
        const float mu = s1 * (1.0f / 32.0f);
        float m2 = 0.f;
#pragma unroll
        for (int j = 0; j < 8; ++j) { const float d = f[j] - mu; m2 += d * d; }
        m2 += __shfl_xor(m2, 8); m2 += __shfl_xor(m2, 16);
        const int part = (kg0 >> 2) + 8 * z;
        const unsigned so = (unsigned)((xtok >> 5) * 14 + part) * 256 + (unsigned)(xtok & 31) * 8;
        stg2(v2u{f2u(s1), f2u(m2)}, rs, ((lane & 24) == 0 && part < 14) ? so : OOB, O_ST);
      }
    }
    sb();
    STAMP(33);
    {
      f32x16 cs = {};
      const v4u ones = {0x3F803F80u, 0x3F803F80u, 0x3F803F80u, 0x3F803F80u};
#pragma unroll
      for (int u = 0; u < 7; ++u) {
        v4u fr;
        fr.x = e[u][0] | (e[u][1] << 16); fr.y = e[u][2] | (e[u][3] << 16);
        fr.z = e[u][4] | (e[u][5] << 16); fr.w = e[u][6] | (e[u][7] << 16);
        if (zlane) { fr.x = 0u; fr.y = 0u; }
        if (zlane || cut) { fr.z = 0u; fr.w = 0u; }
        stg(fr, rs, ldst, dst + u * dstep);
        cs = mfma(fr, ones, cs);
      }
      const unsigned vo = tok == 0 ? hi * 16 : OOB;
#pragma unroll
      for (int qq = 0; qq < 4; ++qq) stg(v4u{f2u(cs[4 * qq]), f2u(cs[4 * qq + 1]), f2u(cs[4 * qq + 2]), f2u(cs[4 * qq + 3])}, rs, vo + qq * 32, csdst);
    }
    STAMP(1);
    drain();
    STAMP(35);
    bar();
    STAMP(36);
    publish(1);
    if (w == 0) { drain(); check(1); }
    bar();
    STAMP(2);
  }

  dma_blocks<21>(p.ws + O_QKVT + c * 84 * 1024, 0, w, lane16);
  v4u wph[2];
  v4u cfa, cfd[2];
  float res[16];
  {
    const rsrc_t rp = __builtin_amdgcn_make_buffer_rsrc((void*)(p.wphi + c * 784), 0, 784 * 2, 0x00020000);
    const unsigned vo = tok < 28 ? (unsigned)(tok + 4 * hi * 28) * 2 : OOB;
    unsigned e[2][8];
#pragma unroll
    for (int s = 0; s < 2; ++s)
#pragma unroll
      for (int j = 0; j < 8; ++j) e[s][j] = (unsigned short)__builtin_amdgcn_raw_buffer_load_b16(rp, vo, (16 * s + 8 * (j >> 2) + (j & 3)) * 56, 0);
    float ca[2][4], cd[4][4];
#pragma unroll
    for (int k = 0; k < 4; ++k) {
      ca[0][k] = u2f(__builtin_amdgcn_raw_buffer_load_b32(rs, tok * 4 + hi * 1024 + k * 128, O_CSQ + c * 1536, 16));
      ca[1][k] = u2f(__builtin_amdgcn_raw_buffer_load_b32(rs, tok * 4 + 512 + k * 128, O_CSQ + c * 1536, 16));
    }
    const unsigned vg = (unsigned)(tok & 15) * 4 + (unsigned)(tok >> 4) * 256 + (unsigned)hi * 1024;
#pragma unroll
    for (int i = 0; i < 4; ++i)
#pragma unroll
      for (int k = 0; k < 4; ++k) cd[i][k] = u2f(__builtin_amdgcn_raw_buffer_load_b32(rs, vg + (i & 1) * 512 + (i >> 1) * 2048 + k * 64, O_CSG + c * 3584, 16));
    const unsigned vr = (unsigned)(tok >> 4) * (56 * 256) + (unsigned)(tok & 15) * 16 + (unsigned)hi * 8;
    v2u rv[4];
#pragma unroll
    for (int qq = 0; qq < 4; ++qq) rv[qq] = ldg2(rs, vr + qq * 256, O_X + S_XST);
    sb();
    STAMP(38);
#pragma unroll
    for (int s = 0; s < 2; ++s) wph[s] = v4u{e[s][0] | (e[s][1] << 16), e[s][2] | (e[s][3] << 16), e[s][4] | (e[s][5] << 16), e[s][6] | (e[s][7] << 16)};
    auto sum4 = [&](const float (&v)[4]) { return (v[0] + v[1]) + (v[2] + v[3]); };
    auto split2 = [&](float x, float y) -> v4u {
      const float x0 = bfr(x), x1 = bfr(x - x0), x2 = (x - x0) - x1;
      const float y0 = bfr(y), y1 = bfr(y - y0), y2 = (y - y0) - y1;
      return v4u{pk(x0, x1), pk(x2, y0), pk(y1, y2), 0u};
    };
    cfa = split2(sum4(ca[0]), hi ? (tok == 28 ? 1.0f : 0.0f) : sum4(ca[1]));
    cfd[0] = split2(sum4(cd[0]), sum4(cd[1]));
    cfd[1] = split2(sum4(cd[2]), hi ? 0.0f : sum4(cd[3]));
#pragma unroll
    for (int qq = 0; qq < 4; ++qq) { res[4 * qq] = blo(rv[qq].x); res[4 * qq + 1] = bhi(rv[qq].x); res[4 * qq + 2] = blo(rv[qq].y); res[4 * qq + 3] = bhi(rv[qq].y); }
  }
  STAMP(39);
  v4u wdn[24][2];

  auto ln_finish = [&](v2u (&st)[7], unsigned vo, unsigned so) -> Stats {
    unsigned m = 0;
#pragma unroll
    for (int i = 0; i < 7; ++i) m = umax(umax(m, st[i].x), st[i].y);
    if (__builtin_expect(__any(m == POISON), 0)) {
#pragma unroll 1
      for (int sp = 0;; ++sp) {
        if (dead || sp > SPIN_CAP) { dead = 1; break; }
        __builtin_amdgcn_s_sleep(1);
        asm volatile("" ::: "memory");
        unsigned n = 0;
#pragma unroll
        for (int i = 0; i < 7; ++i) st[i] = ldg2(rs, vo + i * 512, so);
#pragma unroll
        for (int i = 0; i < 7; ++i) n = umax(umax(n, st[i].x), st[i].y);
        if (!__any(n == POISON)) break;
      }
    }
    const float s = ((u2f(st[0].x) + u2f(st[1].x)) + (u2f(st[2].x) + u2f(st[3].x))) + ((u2f(st[4].x) + u2f(st[5].x)) + u2f(st[6].x));
    const float mean = hsum(s) * (1.0f / 448.0f);
    float m2a = 0.f, m2b = 0.f;
#pragma unroll
    for (int i = 0; i < 7; ++i) { const float d = u2f(st[i].x) * (1.0f / 32.0f) - mean; m2a += u2f(st[i].y); m2b += d * d; }
    const float var = hsum(m2a + 32.0f * m2b) * (1.0f / 448.0f);
    Stats o; o.m = bfr(mean);
    o.s = bfr(__builtin_amdgcn_sqrtf(bfr(bfr(var) + 1e-5f)));
    o.rs = rcp(o.s);
    return o;
  };

  auto red_put = [&](const f32x16& t, int a, unsigned l16) {
    LAS f32x4* d = (LAS f32x4*)(L + L_SCR + ((w ^ a) * 3 + (a - 1)) * 4096 + l16);
#pragma unroll
    for (int qq = 0; qq < 4; ++qq) d[qq * 64] = f32x4{t[4 * qq], t[4 * qq + 1], t[4 * qq + 2], t[4 * qq + 3]};
  };
  auto finish = [&](const f32x16& a0, int xb, bool last) {
    const unsigned ln = opq(lane), l16 = ln * 16;
    bar();
    unsigned o[8];
    f32x4 t[4][3];
#pragma unroll
    for (int qq = 0; qq < 4; ++qq) {
      const LAS f32x4* s = (const LAS f32x4*)(L + L_SCR + (w * 3) * 4096 + l16) + qq * 64;
      t[qq][0] = s[0]; t[qq][1] = s[256]; t[qq][2] = s[512];
    }
#pragma unroll
    for (int i = 0; i < 16; ++i) res[i] += a0[i];
#pragma unroll
    for (int qq = 0; qq < 4; ++qq) {
#pragma unroll
      for (int i = 0; i < 4; ++i) res[4 * qq + i] = (res[4 * qq + i] + t[qq][0][i]) + (t[qq][1][i] + t[qq][2][i]);
      o[2 * qq] = san(pk(res[4 * qq], res[4 * qq + 1])); o[2 * qq + 1] = san(pk(res[4 * qq + 2], res[4 * qq + 3]));
    }
    const v4u p0 = widen(o[0], o[1], o[2], o[3]), p1 = widen(o[4], o[5], o[6], o[7]);
    if (last) {
      if (act) {
        GAS v4u* op = (GAS v4u*)(p.out + (size_t)(32 * T32o + (ln & 31)) * 448 + 64 * cg + 32 * (w & 1) + 8 * (ln >> 5));
        op[0] = p0; op[2] = p1;
      }
      return;
    }
    const unsigned so = act ? O_X + xb * XSZ : O_DUMX, so2 = act ? O_ST + xb * STSZ : O_DUMST;
    { const unsigned v = lxpat(ln, 56 * 256), s2 = so + S_XST; stg(p0, rs, v, s2); stg(p1, rs, v + 512, s2); }
    sb();
    float s1a = 0.f, s1b = 0.f;
#pragma unroll
    for (int qq = 0; qq < 8; ++qq) { res[2 * qq] = blo(o[qq]); res[2 * qq + 1] = bhi(o[qq]); s1a += res[2 * qq]; s1b += res[2 * qq + 1]; }
    const float s1 = hsum(s1a + s1b);
    const float mu = s1 * (1.0f / 32.0f);
    float m2a = 0.f, m2b = 0.f;
#pragma unroll
    for (int i = 0; i < 8; ++i) { const float d = res[2 * i] - mu, e = res[2 * i + 1] - mu; m2a += d * d; m2b += e * e; }
    const float m2 = hsum(m2a + m2b);
    stg2(v2u{san(f2u(s1)), san(f2u(m2))}, rs, ln < 32 ? ln * 8 : OOB, so2 + S_STST);
  };

  auto dual_softmax = [&](const f32x16& a, float sc, float osc, unsigned (&o)[16]) {
    float mx = fmaxf(fmaxf(a[0], a[1]), a[2]), mn = fminf(fminf(a[0], a[1]), a[2]);
    float mxb = fmaxf(fmaxf(a[3], a[4]), a[5]), mnb = fminf(fminf(a[3], a[4]), a[5]);
    mx = fmaxf(fmaxf(mx, a[6]), a[7]); mn = fminf(fminf(mn, a[6]), a[7]);
    mxb = fmaxf(fmaxf(mxb, a[8]), a[9]); mnb = fminf(fminf(mnb, a[8]), a[9]);
    mx = fmaxf(fmaxf(mx, a[10]), a[11]); mn = fminf(fminf(mn, a[10]), a[11]);
    float mx2 = fmaxf(fmaxf(a[12], a[13]), fmaxf(a[14], a[15])), mn2 = fminf(fminf(a[12], a[13]), fminf(a[14], a[15]));
    if (hi) { mx2 = a[0]; mn2 = a[0]; }
    mx = hmax(fmaxf(fmaxf(mx, mxb), mx2)); mn = hmin(fminf(fminf(mn, mnb), mn2));
    const float k = sc * 1.4426950408889634f;
    const float cx = -mx * k, cn = mn * k;
    float ep[16], en[16];
#pragma unroll
    for (int i = 0; i < 16; ++i) {
      ep[i] = __builtin_amdgcn_exp2f(__builtin_fmaf(a[i], k, cx));
      en[i] = __builtin_amdgcn_exp2f(__builtin_fmaf(a[i], -k, cn));
      if (i >= 12 && hi) { ep[i] = 0.f; en[i] = 0.f; }
    }
    float sp[4], sn[4];
#pragma unroll
    for (int i = 0; i < 4; ++i) { sp[i] = (ep[i] + ep[4 + i]) + (ep[8 + i] + ep[12 + i]); sn[i] = (en[i] + en[4 + i]) + (en[8 + i] + en[12 + i]); }
    const float rp = osc * rcp(hsum((sp[0] + sp[1]) + (sp[2] + sp[3]))), rn = osc * rcp(hsum((sn[0] + sn[1]) + (sn[2] + sn[3])));
#pragma unroll
    for (int i = 0; i < 8; ++i) { o[i] = pk(ep[2 * i] * rp, ep[2 * i + 1] * rp); o[8 + i] = pk(en[2 * i] * rn, en[2 * i + 1] * rn); }
  };
#define LDSF(kb) (*(const LAS v4u*)(L + ((kb) < 64 ? lane16 + (kb) * 1024 : (kb) < 128 ? lane16b + ((kb) - 64) * 1024 : lane16c + ((kb) - 128) * 1024)))
#define XLOADS(so, so2)                                                                                        \
  do {                                                                                                         \
    _Pragma("unroll") for (int s = 0; s < 16; ++s) xf[s] = ldg(rs, vx_a + (s & 7) * 512, (so) + (s >> 3) * 4096); \
    _Pragma("unroll") for (int i = 0; i < 7; ++i) st[i] = ldg2(rs, vst + i * 512, (so2));                      \
    _Pragma("unroll") for (int s = 16; s < 28; ++s) xf[s] = ldg(rs, vx_a + (s & 7) * 512, (so) + (s >> 3) * 4096); \
    sb();                                                                                                      \
  } while (0)

#pragma unroll 1
  for (int l = 0; l < 2; ++l) {
    v4u xf[28];
    v2u st[7];
    const unsigned lane16 = opq(lane) * 16;
    const unsigned lane16b = opq(lane16 + 65536), lane16c = opq(lane16 + 131072);
    const unsigned vx_a = lxpat(opq(lane), 56 * 256);
    const unsigned vst = lstpat(opq(lane));
    { const unsigned so = S_XA(2 * l), so2 = S_STA(2 * l); XLOADS(so, so2); }
    if (l == 0) drain();
    bar();
    STAMP(3 + 16 * l);
    f32x16 ak = {}, av = {}, aq = {};
    Stats sa;
    {
      const unsigned so = S_XA(2 * l);
      v4u wb[2][3];
#pragma unroll
      for (int s = 0; s < 2; ++s)
#pragma unroll
        for (int t = 0; t < 3; ++t) wb[s][t] = LDSF(3 * s + t);
#pragma unroll
      for (int g = 0; g < 7; ++g) {
#define REF_(i) xf[4 * g + (i)]
#define REL_(i) xf[4 * g + (i)] = ldg(rs, vx_a + ((4 * g + (i)) & 7) * 512, so + ((4 * g + (i)) >> 3) * 4096)
        VALIDATE(4, REF_, REL_);
#undef REF_
#undef REL_
#pragma unroll
        for (int i = 0; i < 4; ++i) {
          const int s = 4 * g + i;
          ak = mfma(wb[s & 1][0], xf[s], ak);
          if (s + 2 < 28) wb[s & 1][0] = LDSF(3 * (s + 2));
          av = mfma(xf[s], wb[s & 1][1], av);
          if (s + 2 < 28) wb[s & 1][1] = LDSF(3 * (s + 2) + 1);
          aq = mfma(wb[s & 1][2], xf[s], aq);
          if (s + 2 < 28) wb[s & 1][2] = LDSF(3 * (s + 2) + 2);
          if (i < 3) sb();
        }
        if (g == 4) sa = ln_finish(st, vst, S_STA(2 * l));
      }
    }
    STAMP(4 + 16 * l);
    const unsigned nm2 = pk(-sa.m, -sa.m);
    {
      LAS unsigned char* const SW = L + L_SCR + w * 6400;
      ak = mfma(cfa, hi ? v4u{0u, 0u, 0u, 0u} : v4u{0u, nm2 & 0xFFFF0000u, nm2, 0u}, ak);
      av = mfma(hi ? v4u{nm2, pk(-sa.m, sa.s), 0u, 0u} : v4u{0u, 0u, 0u, 0u}, cfa, av);
      aq = mfma(cfa, hi ? v4u{0u, 0u, 0u, 0u} : v4u{nm2, nm2 & 0xFFFFu, 0u, 0u}, aq);
      f32x16 pkk = {};
      pkk = mfma(wph[0], v4u{pk(ak[0], ak[1]), pk(ak[2], ak[3]), pk(ak[4], ak[5]), pk(ak[6], ak[7])}, pkk);
      pkk = mfma(wph[1], v4u{pk(ak[8], ak[9]), pk(ak[10], ak[11]), pk(ak[12], ak[13]), pk(ak[14], ak[15])}, pkk);
      *(LAS v4u*)(SW + 4352 + lane16) = v4u{pk(av[0], av[1]), pk(av[2], av[3]), pk(av[4], av[5]), pk(av[6], av[7])};
      *(LAS v4u*)(SW + 5376 + lane16) = v4u{pk(av[8], av[9]), pk(av[10], av[11]), pk(av[12], av[13]), pk(av[14], av[15])};
      unsigned ph[16];
      dual_softmax(pkk, sa.rs, sa.rs, ph);
      const unsigned ln2 = opq(lane), phw = (ln2 & 31) * 136 + (ln2 >> 5) * 8;
#pragma unroll
      for (int t = 0; t < 2; ++t)
#pragma unroll
        for (int qq = 0; qq < 4; ++qq) *(LAS v2u*)(SW + phw + t * 64 + qq * 16) = v2u{ph[8 * t + 2 * qq], ph[8 * t + 2 * qq + 1]};
      if (l == 1 && w == 3) check(3);
      bar();
      if (w < 2) {
        const unsigned ln = opq(lane), G = ln >> 4, q4 = (ln & 15) >> 2, p4 = ln & 3;
        const LAS unsigned char* tb = L + L_SCR + (4 * (G >> 1) + q4) * 136 + (16 * (G & 1) + 4 * p4) * 2 + w * 64;
        v4u fa[8], fb[8];
#pragma unroll
        for (int i = 0; i < 8; ++i) {
          const int ww = i >> 1, s = i & 1;
          const s16x4 lo = __builtin_amdgcn_ds_read_tr16_b64_v4i16((LAS s16x4*)(tb + ww * 6400 + (16 * s) * 136));
          const s16x4 hh = __builtin_amdgcn_ds_read_tr16_b64_v4i16((LAS s16x4*)(tb + ww * 6400 + (16 * s + 8) * 136));
          fa[i] = __builtin_bit_cast(v4u, __builtin_shufflevector(lo, hh, 0, 1, 2, 3, 4, 5, 6, 7));
          fb[i] = *(const LAS v4u*)(L + L_SCR + ww * 6400 + 4352 + s * 1024 + lane16);
        }
        f32x16 kv = {};
#pragma unroll
        for (int i = 0; i < 8; ++i) kv = mfma(fa[i], fb[i], kv);
        const unsigned so = S_KVP(l) + w * 4096;
#pragma unroll
        for (int qq = 0; qq < 4; ++qq)
          stg(v4u{san(f2u(kv[4 * qq])), san(f2u(kv[4 * qq + 1])), san(f2u(kv[4 * qq + 2])), san(f2u(kv[4 * qq + 3]))}, rs, lane16 + qq * 1024, so);
      }
    }
    STAMP(5 + 16 * l);
    dma_blocks<28>(p.ws + O_GUT + (opqs(c) * 7) * 28 * 1024, 0, w, lane16);
    unsigned pq[16];
    v4u wf[8][2];
    {
      f32x16 pp = {};
      pp = mfma(wph[0], v4u{pk(aq[0], aq[1]), pk(aq[2], aq[3]), pk(aq[4], aq[5]), pk(aq[6], aq[7])}, pp);
      pp = mfma(wph[1], v4u{pk(aq[8], aq[9]), pk(aq[10], aq[11]), pk(aq[12], aq[13]), pk(aq[14], aq[15])}, pp);
      dual_softmax(pp, sa.rs, 1.0f, pq);
      STAMP(6 + 16 * l);
      if (w == 2) {
        const int fr = opqs(r) >> 2, lq = r & 3;
        const unsigned ln = opq(lane), vo = (ln >> 4) * 8192 + (ln & 15) * 16;
        v4u pa[8];
        const unsigned so = O_KVP + l * KVPSZ + (unsigned)(opqs(c) * 128 + 2 * fr) * 1024 + (unsigned)lq * 256;
#define REF_(i) pa[i]
#define REL_(i) pa[i] = ldg(rs, vo + ((i) & 1) * 1024, so + ((i) >> 1) * 32768)
#pragma unroll
        for (int i = 0; i < 8; ++i) REL_(i);
        VALIDATE(8, REF_, REL_);
#undef REF_
#undef REL_
        float sv[8];
#pragma unroll
        for (int z = 0; z < 2; ++z)
#pragma unroll
          for (int i = 0; i < 4; ++i) {
            float t = (u2f(pa[z][i]) + u2f(pa[2 + z][i])) + (u2f(pa[4 + z][i]) + u2f(pa[6 + z][i]));
            auto r16 = __builtin_amdgcn_permlane16_swap(f2u(t), f2u(t), false, false);
            t = u2f(r16[0]) + u2f(r16[1]);
            sv[4 * z + i] = hsum(t);
          }
        const v4u o = {san(pk(sv[0], sv[1])), san(pk(sv[2], sv[3])), san(pk(sv[4], sv[5])), san(pk(sv[6], sv[7]))};
        stg(o, rs, ln < 16 ? ln * 16 : OOB, S_KVRST(l));
      }
      STAMP(15 + 16 * l);
      const unsigned swo = O_WOT + (unsigned)(opqs(cg) * 4 + w) * 16384;
#pragma unroll
      for (int ks = 0; ks < 8; ++ks)
#pragma unroll
        for (int n = 0; n < 2; ++n) wf[ks][n] = ldg(rs, lane16 + (ks & 1) * 2048, swo + (unsigned)(n ^ (w & 1)) * 1024 + (ks >> 1) * 4096);
      if (l == 0) {
        poison_h(0);
        const unsigned ln = opq(lane), v56 = act ? lxpat(ln, 56 * 256) : OOB;
        stg(P4, rs, v56, O_X + 2 * XSZ + S_XST); stg(P4, rs, v56 + 512, O_X + 2 * XSZ + S_XST);
        stg2(P2, rs, (act && ln < 32) ? ln * 8 : OOB, O_ST + 2 * STSZ + S_STST);
      }
    }
    drain();
    bar();
    STAMP(7 + 16 * l);
    {
      v4u kf[4];
      const unsigned so = O_KVR + l * KVRSZ + (unsigned)opqs(c) * 4096;
#define REF_(i) kf[i]
#define REL_(i) kf[i] = ldg(rs, lane16 + (i) * 1024, so)
#pragma unroll
      for (int i = 0; i < 4; ++i) REL_(i);
      if (l == 0) publish(2);
      VALIDATE(4, REF_, REL_);
#undef REF_
#undef REL_
      f32x16 nm = {};
#pragma unroll
      for (int i = 0; i < 4; ++i) nm = mfma(kf[i], v4u{pq[4 * i], pq[4 * i + 1], pq[4 * i + 2], pq[4 * i + 3]}, nm);
      auto rd = __builtin_amdgcn_permlane32_swap(f2u(nm[12]), f2u(nm[12]), false, false);
      const float den = bfr(bfr(u2f(rd[1])) + 1e-6f);
      const float rden = rcp(den);
      unsigned o[8];
#pragma unroll
      for (int i = 0; i < 8; ++i) o[i] = san(pk(nm[2 * i] * rden, nm[2 * i + 1] * rden));
      if (hi) { o[6] = 0u; o[7] = 0u; }
      const unsigned v64 = lxpat(opq(lane), 64 * 256), s2 = S_OFST(l);
      stg(widen(o[0], o[1], o[2], o[3]), rs, v64, s2);
      stg(widen(o[4], o[5], o[6], o[7]), rs, v64 + 512, s2);
    }
    STAMP(8 + 16 * l);
    drain();
    {
      v4u of[8][2];
      const unsigned vo = lxpat(opq(lane), 64 * 256);
      const unsigned so0 = O_OF + l * OFSZ + (unsigned)(16 * w) * 256;
      const unsigned so[2] = {so0 + (unsigned)opqs(T32o) * (2 * 64 * 256), so0 + (unsigned)(opqs(T32o) ^ 1) * (2 * 64 * 256)};
#pragma unroll
      for (int ks = 0; ks < 8; ++ks)
#pragma unroll
        for (int t = 0; t < 2; ++t) of[ks][t] = ldg(rs, vo + ks * 512, so[t]);
      sb();
      f32x16 acc[4] = {};
#pragma unroll
      for (int g = 0; g < 4; ++g) {
#define REF_(i) of[2 * g + ((i) >> 1)][(i) & 1]
#define REL_(i) of[2 * g + ((i) >> 1)][(i) & 1] = ldg(rs, vo + (2 * g + ((i) >> 1)) * 512, so[(i) & 1])
        VALIDATE(4, REF_, REL_);
#undef REF_
#undef REL_
        if (g < 3) {
#pragma unroll
          for (int k2 = 0; k2 < 2; ++k2)
#pragma unroll
            for (int a = 0; a < 4; ++a) acc[a] = mfma(wf[2 * g + k2][a & 1], of[2 * g + k2][a >> 1], acc[a]);
        } else {
#pragma unroll
          for (int aa = 1; aa < 5; ++aa) {
            const int a = aa & 3;
#pragma unroll
            for (int k2 = 0; k2 < 2; ++k2) acc[a] = mfma(wf[2 * g + k2][a & 1], of[2 * g + k2][a >> 1], acc[a]);
            if (aa > 1) red_put(acc[aa - 1], aa - 1, lane16);
            sb();
          }
        }
      }
      STAMP(9 + 16 * l);
      finish(acc[0], 2 * l + 1, false);
    }
    bar();
    STAMP(10 + 16 * l);
    dma_blocks<7>(p.ws + O_GUT + (opqs(c) * 7 + 4) * 28 * 1024, 112 * 1024, w, lane16);
    drain();
    const unsigned vh = lxpat(opq(lane), 224 * 256);
    const unsigned sh0 = O_HB + l * HBSZ + (unsigned)(56 * w) * 256;
    const unsigned sh[2] = {sh0 + (unsigned)opqs(T32o) * (2 * 224 * 256), sh0 + (unsigned)(opqs(T32o) ^ 1) * (2 * 224 * 256)};
    v4u hf[4][8];
#define HLOAD(g, i) hf[(g) % 4][i] = ldg(rs, vh + (g) * 512, sh[(i) & 1] + ((i) >> 1) * (14 * 256))
    {
      const unsigned so = S_XA(2 * l + 1), so2 = S_STA(2 * l + 1);
      XLOADS(so, so2);
      bar();
      STAMP(11 + 16 * l);
      f32x16 ac[7];
      ac[0] = f32x16{}; ac[1] = f32x16{}; ac[2] = f32x16{};
      Stats sd;
      {
        v4u wb[2][3];
#pragma unroll
        for (int s = 0; s < 2; ++s)
#pragma unroll
          for (int t = 0; t < 3; ++t) wb[s][t] = LDSF(28 * t + s);
#pragma unroll
        for (int g = 0; g < 7; ++g) {
#define REF_(i) xf[4 * g + (i)]
#define REL_(i) xf[4 * g + (i)] = ldg(rs, vx_a + ((4 * g + (i)) & 7) * 512, so + ((4 * g + (i)) >> 3) * 4096)
          VALIDATE(4, REF_, REL_);
#undef REF_
#undef REL_
#pragma unroll
          for (int i = 0; i < 4; ++i) {
            const int s = 4 * g + i;
#pragma unroll
            for (int t = 0; t < 3; ++t) {
              ac[t] = mfma(wb[s & 1][t], xf[s], ac[t]);
              if (s + 2 < 28) wb[s & 1][t] = LDSF(28 * t + s + 2);
            }
            if (i < 3) sb();
          }
          if (g == 4) sd = ln_finish(st, vst, so2);
        }
      }
      STAMP(41);
      const unsigned shb = S_HBST(l);
      const float nrs = -1.4426950408889634f * sd.rs;
      const unsigned dm2 = pk(-sd.m, -sd.m);
      auto corr = [&](int t) -> v4u {
        const bool on = ((t >> 1) & 1) == hi;
        const unsigned z = on ? dm2 : 0u;
        return (t & 1) ? v4u{0u, z & 0xFFFF0000u, z, 0u} : v4u{z, z & 0xFFFFu, 0u, 0u};
      };
#pragma unroll
      for (int t = 0; t < 3; ++t) ac[t] = mfma(cfd[0], corr(t), ac[t]);
      if (l == 0 && w == 3) check(2);
      bar();
      STAMP(42);
      dma_blocks<14>(p.ws + O_GUT + (opqs(c) * 7 + 5) * 28 * 1024, 0, w, lane16);
      if (l == 0) {
        poison_attn(1); poison_h(1);
        const unsigned sdn = O_DNT + (unsigned)(opqs(cg) * 4 + w) * (56 * 1024);
#pragma unroll
        for (int s = 0; s < 24; ++s)
#pragma unroll
          for (int n = 0; n < 2; ++n) wdn[s][n] = ldg(rs, lane16 + (s & 1) * 2048, sdn + (unsigned)(n ^ (w & 1)) * 1024 + (s >> 1) * 4096);
      }
      asm volatile("s_nop 15\n\ts_nop 7" ::: "memory");
      float tg[4], tu[4]; unsigned ho[4];
#define PIN(x) asm volatile("" : "+v"(x))
      auto epi_item = [&](auto N) {
        constexpr int n = decltype(N)::value, t = n / 18, k = n % 18, e0 = k < 8 ? 0 : 4, kk = k & 7;
        if constexpr (k < 16) {
          if constexpr (kk == 0) { _Pragma("unroll") for (int i = 0; i < 4; ++i) { tg[i] = ac[t][e0 + i]; PIN(tg[i]); } }
          else if constexpr (kk == 1) { _Pragma("unroll") for (int i = 0; i < 4; ++i) { tg[i] *= nrs; PIN(tg[i]); } }
          else if constexpr (kk == 2 || kk == 3) { _Pragma("unroll") for (int i = 2 * (kk - 2); i < 2 * (kk - 2) + 2; ++i) { tg[i] = __builtin_amdgcn_exp2f(tg[i]); PIN(tg[i]); tu[i] = ac[t][8 + e0 + i]; PIN(tu[i]); } }
          else if constexpr (kk == 4) { _Pragma("unroll") for (int i = 0; i < 4; ++i) { tg[i] = __builtin_fmaf(tg[i], sd.s, sd.s); PIN(tg[i]); } }
          else if constexpr (kk == 5) { _Pragma("unroll") for (int i = 0; i < 2; ++i) { tg[i] = rcp(tg[i]); PIN(tg[i]); } }
          else if constexpr (kk == 6) { _Pragma("unroll") for (int i = 0; i < 2; ++i) { tg[2 + i] = rcp(tg[2 + i]); PIN(tg[2 + i]); tu[i] *= tg[i]; PIN(tu[i]); } }
          else { tu[2] *= tg[2]; tu[3] *= tg[3]; ho[e0 >> 1] = pk(tu[0], tu[1]); ho[(e0 >> 1) + 1] = pk(tu[2], tu[3]); PIN(ho[e0 >> 1]); PIN(ho[(e0 >> 1) + 1]); }
        } else if constexpr (k == 16) { _Pragma("unroll") for (int i = 0; i < 4; ++i) { ho[i] = san(ho[i]); PIN(ho[i]); } }
        else stg(widen(ho[0], ho[1], ho[2], ho[3]), rs, vh + t * 512, shb);
      };
      sfor<3, 7>([&](auto T) {
        constexpr int t = decltype(T)::value;
        STAMP(40 + 2 * t - 3);
        if constexpr (t == 5) { drain(); bar(); }
        STAMP(40 + 2 * t - 2);
        constexpr int kb = (t < 5 ? t : t - 5) * 28;
        v4u wb[5];
#pragma unroll
        for (int s = 0; s < 5; ++s) wb[s] = LDSF(kb + s);
        ac[t] = mfma(cfd[t >> 2], corr(t), f32x16{});
        sfor<0, 28>([&](auto S) {
          constexpr int s = decltype(S)::value;
          ac[t] = mfma(wb[s % 5], xf[s], ac[t]);
          if constexpr (s + 5 < 28) wb[s % 5] = LDSF(kb + s + 5);
          constexpr int n = (t - 3) * 28 + s;
          if constexpr (n < 6 * 18 && n / 18 < t) epi_item(IC<n>{});
          if constexpr (t == 6 && s >= 8) { constexpr int hs = s - 8; HLOAD(hs >> 3, hs & 7); }
          sb();
        });
      });
      STAMP(51);
#pragma unroll
      for (int i = 4; i < 8; ++i) HLOAD(2, i);
#pragma unroll
      for (int i = 0; i < 8; ++i) HLOAD(3, i);
      sb();
      sfor<0, 18>([&](auto K) { epi_item(IC<6 * 18 + decltype(K)::value>{}); });
    }
    STAMP(12 + 16 * l);
    if (l == 0) publish(3);
    {
      f32x16 acc[4] = {};
      v4u wd6[4][2];
      {
        const unsigned sdn = O_DNT + (unsigned)(opqs(cg) * 4 + w) * (56 * 1024) + 48 * 1024;
#pragma unroll
        for (int s = 0; s < 4; ++s)
#pragma unroll
          for (int n = 0; n < 2; ++n) wd6[s][n] = ldg(rs, lane16 + (s & 1) * 2048, sdn + (unsigned)(n ^ (w & 1)) * 1024 + (s >> 1) * 4096);
        sb();
      }
#define REF_(i) hf[0][i]
#define REL_(i) HLOAD(0, i)
      VALIDATE(8, REF_, REL_);
#undef REF_
#undef REL_
#pragma unroll
      for (int g = 0; g < 7; ++g) {
        STAMP(53 + g);
        unsigned m0 = 0, m1 = 0;
        if (g < 6) {
#pragma unroll
          for (int i = 0; i < 16; ++i) {
            const int a = i & 3, k = i >> 2;
            acc[a] = mfma(wdn[(g < 6 ? g : 0) * 4 + k][a & 1], hf[g % 4][2 * k + (a >> 1)], acc[a]);
            if (i >= 8) { const v4u v = hf[(g + 1) % 4][i - 8]; m0 = umax(umax(m0, v.x), v.y); m1 = umax(umax(m1, v.z), v.w); }
            sb();
          }
          if (g + 4 < 7) {
#pragma unroll
            for (int i = 0; i < 8; ++i) HLOAD(g + 4, i);
          }
          if (__builtin_expect(__any(umax(m0, m1) == POISON), 0)) {
#define REF_(i) hf[(g + 1) % 4][i]
#define REL_(i) HLOAD(g + 1, i)
            VALIDATE(8, REF_, REL_);
#undef REF_
#undef REL_
          }
        } else {
          bar();
          if (l == 0) dma_blocks<21>(p.ws + O_QKVT + opqs(c) * 84 * 1024, 0, w, lane16);
#pragma unroll
          for (int aa = 1; aa < 5; ++aa) {
            const int a = aa & 3;
#pragma unroll
            for (int k = 0; k < 4; ++k) acc[a] = mfma(wd6[k][a & 1], hf[g % 4][2 * k + (a >> 1)], acc[a]);
            if (aa > 1) red_put(acc[aa - 1], aa - 1, lane16);
            sb();
          }
        }
      }
#undef HLOAD
      STAMP(13 + 16 * l);
      finish(acc[0], 2, l == 1);
    }
    STAMP(14 + 16 * l);
    asm volatile("s_waitcnt vmcnt(1)" ::: "memory");
  }
  if (b == 0 && tid == 0) *(GAS u64*)(p.ws + O_CTRL) = epoch + 1ull;
  STAMP(61); STAMPC(63);
}

}

extern "C" void kernel_launch(void* const* d_in, const int* in_sizes, int n_in, void* d_out, int out_size, void* d_ws,
                              size_t ws_size, hipStream_t stream) {
  Params p;
  p.x = (const unsigned short*)d_in[0];
  p.wqkv = (const unsigned short*)d_in[1];
  p.wphi = (const unsigned short*)d_in[2];
  p.wo = (const unsigned short*)d_in[3];
  p.wg = (const unsigned short*)d_in[5];
  p.wu = (const unsigned short*)d_in[6];
  p.wd = (const unsigned short*)d_in[7];
  p.out = (unsigned short*)d_out;
  p.ws = (unsigned char*)d_ws;
  hipFuncSetAttribute((const void*)mega, hipFuncAttributeMaxDynamicSharedMemorySize, LDS_BYTES);
  hipLaunchKernelGGL(mega, dim3(256), dim3(256), LDS_BYTES, stream, p);
}
```

```cpp
#include <hip/hip_runtime.h>
#include <stdint.h>

namespace {

typedef unsigned v4u __attribute__((ext_vector_type(4)));
typedef unsigned v2u __attribute__((ext_vector_type(2)));
typedef float f32x4 __attribute__((ext_vector_type(4)));
typedef float f32x2 __attribute__((ext_vector_type(2)));
typedef float f32x16 __attribute__((ext_vector_type(16)));
typedef short bf16x8 __attribute__((ext_vector_type(8)));
typedef short s16x4 __attribute__((ext_vector_type(4)));
typedef __bf16 bf16x2 __attribute__((ext_vector_type(2)));
typedef unsigned long long u64;
#define LAS __attribute__((address_space(3)))
#define GAS __attribute__((address_space(1)))
#define DI __device__ __forceinline__
typedef __amdgpu_buffer_rsrc_t rsrc_t;

#define STAMPS 0
#define LOOKA 4
#define HST 92

constexpr unsigned O_CTRL = 0;
constexpr unsigned O_FLG = 4096;
constexpr unsigned O_QKVI = 32768;
constexpr unsigned O_WOT = O_QKVI + 16 * 84 * 1024;
constexpr unsigned O_GUT = O_WOT + 448 * 1024;
constexpr unsigned O_DNT = O_GUT + 3136 * 1024;
constexpr unsigned O_CSG = O_DNT + 1568 * 1024;
constexpr unsigned XSZ = 128 * 56 * 256;
constexpr unsigned O_X = O_CSG + 131072;
constexpr unsigned STSZ = 64 * 14 * 256;
constexpr unsigned O_ST = O_X + 4 * XSZ;
constexpr unsigned KVPSZ = 16 * 16 * 8192;
constexpr unsigned O_KVP = O_ST + 4 * STSZ;
constexpr unsigned KVRSZ = 16 * 4096;
constexpr unsigned O_KVR = O_KVP + 2 * KVPSZ;
constexpr unsigned OFSZ = 128 * 64 * 256;
constexpr unsigned O_OF = O_KVR + 2 * KVRSZ;
constexpr unsigned HBSZ = 128 * 224 * 256;
constexpr unsigned O_HB = O_OF + 2 * OFSZ;
constexpr unsigned O_DUMX = O_HB + 2 * HBSZ;
constexpr unsigned O_DUMST = O_DUMX + XSZ;
constexpr unsigned O_STAMP = O_DUMST + STSZ;
constexpr unsigned WS_BYTES = O_STAMP + 256 * 4 * 64 * 8;

constexpr int L_SIG = 111 * 1024;
constexpr int L_SCR = 112 * 1024;
constexpr int L_SUM = 148 * 1024;
constexpr int LDS_BYTES = 160 * 1024;
enum { P_QW = 9, P_J = 10, P_Q = 17, P_G2 = 18, P_T2D = 19, P_T4 = 20, P_T56 = 21, P_DD = 22 };

constexpr unsigned POISON = 0xFFFFFFFFu;
constexpr int SPIN_CAP = 1 << 16;

struct Params {
  const unsigned short* x;
  const unsigned short* wqkv;
  const unsigned short* wphi;
  const unsigned short* wo;
  const unsigned short* wg;
  const unsigned short* wu;
  const unsigned short* wd;
  unsigned short* out;
  unsigned char* ws;
};

DI v4u ldg(rsrc_t rs, unsigned voff, unsigned soff) { return __builtin_amdgcn_raw_buffer_load_b128(rs, voff, soff, 16); }
DI v2u ldg2(rsrc_t rs, unsigned voff, unsigned soff) { return __builtin_amdgcn_raw_buffer_load_b64(rs, voff, soff, 16); }
DI void stg(v4u v, rsrc_t rs, unsigned voff, unsigned soff) { __builtin_amdgcn_raw_buffer_store_b128(v, rs, voff, soff, 16); }
DI void stg2(v2u v, rsrc_t rs, unsigned voff, unsigned soff) { __builtin_amdgcn_raw_buffer_store_b64(v, rs, voff, soff, 16); }
DI unsigned san(unsigned x) { return x < 0xFFFFFFFEu ? x : 0xFFFFFFFEu; }
DI v4u san4(v4u v) { return v4u{san(v.x), san(v.y), san(v.z), san(v.w)}; }
DI unsigned pk(float a, float b) { f32x2 v = {a, b}; return __builtin_bit_cast(unsigned, __builtin_convertvector(v, bf16x2)); }
DI float blo(unsigned u) { return __builtin_bit_cast(float, u << 16); }
DI float bhi(unsigned u) { return __builtin_bit_cast(float, u & 0xFFFF0000u); }
DI float bfr(float a) { return blo(pk(a, a)); }
DI float u2f(unsigned u) { return __builtin_bit_cast(float, u); }
DI unsigned f2u(float f) { return __builtin_bit_cast(unsigned, f); }
DI f32x16 mfma(v4u a, v4u b, f32x16 c) {
  return __builtin_amdgcn_mfma_f32_32x32x16_bf16(__builtin_bit_cast(bf16x8, a), __builtin_bit_cast(bf16x8, b), c, 0, 0, 0);
}
DI unsigned umax(unsigned a, unsigned b) { return a > b ? a : b; }
DI unsigned mx4(unsigned m, v4u v) { return umax(umax(umax(m, v.x), v.y), umax(v.z, v.w)); }
DI void bar() { asm volatile("s_waitcnt lgkmcnt(0)" ::: "memory"); __builtin_amdgcn_s_barrier(); asm volatile("" ::: "memory"); }
DI void drain() { asm volatile("s_waitcnt vmcnt(0)" ::: "memory"); }
DI void sb() { __builtin_amdgcn_sched_barrier(0); }
DI unsigned opq(unsigned v) { asm volatile("" : "+v"(v)); return v; }
DI int opqs(int v) { asm volatile("" : "+s"(v)); return v; }
DI float rcp(float x) { return __builtin_amdgcn_rcpf(x); }
DI float hsum(float x) { auto r = __builtin_amdgcn_permlane32_swap(f2u(x), f2u(x), false, false); return u2f(r[0]) + u2f(r[1]); }
DI float hmax(float x) { auto r = __builtin_amdgcn_permlane32_swap(f2u(x), f2u(x), false, false); return fmaxf(u2f(r[0]), u2f(r[1])); }
DI float hmin(float x) { auto r = __builtin_amdgcn_permlane32_swap(f2u(x), f2u(x), false, false); return fminf(u2f(r[0]), u2f(r[1])); }
DI v4u widen(unsigned ax, unsigned ay, unsigned bx, unsigned by) {
  auto r0 = __builtin_amdgcn_permlane32_swap(ax, bx, false, false);
  auto r1 = __builtin_amdgcn_permlane32_swap(ay, by, false, false);
  return v4u{r0[0], r1[0], r0[1], r1[1]};
}
template <int N> DI void dmak(const unsigned char* g, unsigned l, unsigned voff) {
  unsigned keep;
  if constexpr (N == 1)
    asm volatile("s_mov_b32 %0, m0\n\ts_mov_b32 m0, %1\n\ts_nop 0\n\tglobal_load_lds_dwordx4 %2, %3 sc1\n\ts_mov_b32 m0, %0"
                 : "=&s"(keep) : "s"(l), "v"(voff), "s"(g) : "memory");
  else if constexpr (N == 2)
    asm volatile("s_mov_b32 %0, m0\n\ts_mov_b32 m0, %1\n\ts_nop 0\n\tglobal_load_lds_dwordx4 %2, %3 sc1\n\t"
                 "global_load_lds_dwordx4 %2, %3 offset:1024 sc1\n\ts_mov_b32 m0, %0"
                 : "=&s"(keep) : "s"(l), "v"(voff), "s"(g) : "memory");
  else if constexpr (N == 3)
    asm volatile("s_mov_b32 %0, m0\n\ts_mov_b32 m0, %1\n\ts_nop 0\n\tglobal_load_lds_dwordx4 %2, %3 sc1\n\t"
                 "global_load_lds_dwordx4 %2, %3 offset:1024 sc1\n\tglobal_load_lds_dwordx4 %2, %3 offset:2048 sc1\n\ts_mov_b32 m0, %0"
                 : "=&s"(keep) : "s"(l), "v"(voff), "s"(g) : "memory");
  else
    asm volatile("s_mov_b32 %0, m0\n\ts_mov_b32 m0, %1\n\ts_nop 0\n\tglobal_load_lds_dwordx4 %2, %3 sc1\n\t"
                 "global_load_lds_dwordx4 %2, %3 offset:1024 sc1\n\tglobal_load_lds_dwordx4 %2, %3 offset:2048 sc1\n\t"
                 "global_load_lds_dwordx4 %2, %3 offset:3072 sc1\n\ts_mov_b32 m0, %0"
                 : "=&s"(keep) : "s"(l), "v"(voff), "s"(g) : "memory");
}
DI void dmab(v4u rsrc, unsigned voff, unsigned soff, unsigned l) {
  unsigned keep;
  asm volatile("s_mov_b32 %0, m0\n\ts_mov_b32 m0, %1\n\ts_nop 0\n\tbuffer_load_dwordx4 %2, %3, %4 offen lds\n\ts_mov_b32 m0, %0"
               : "=&s"(keep) : "s"(l), "v"(voff), "s"(rsrc), "s"(soff) : "memory");
}
DI v4u mkrs(const void* p, unsigned bytes) {
  return v4u{(unsigned)__builtin_amdgcn_readfirstlane((unsigned)(u64)p), (unsigned)__builtin_amdgcn_readfirstlane((unsigned)((u64)p >> 32)) & 0xFFFFu,
             (unsigned)__builtin_amdgcn_readfirstlane(bytes), 0x00020000u};
}
DI float dot2(unsigned a, unsigned b, float c) { return __builtin_amdgcn_fdot2_f32_bf16(__builtin_bit_cast(bf16x2, a), __builtin_bit_cast(bf16x2, b), c, false); }
DI v4u trf(const LAS unsigned char* a, int d) {
  const s16x4 lo = __builtin_amdgcn_ds_read_tr16_b64_v4i16((LAS s16x4*)a);
  const s16x4 hh = __builtin_amdgcn_ds_read_tr16_b64_v4i16((LAS s16x4*)(a + d));
  return __builtin_bit_cast(v4u, __builtin_shufflevector(lo, hh, 0, 1, 2, 3, 4, 5, 6, 7));
}
template <int N> DI void vmwait() { asm volatile("s_waitcnt vmcnt(%0)" :: "n"(N) : "memory"); }
DI void rawbar() { asm volatile("" ::: "memory"); __builtin_amdgcn_s_barrier(); asm volatile("" ::: "memory"); }

#define RETRY(i) do { } while (0)
#define STAMP(i) do { } while (0)
#define STAMPC(i) do { } while (0)

#define STAMPD(i) do { } while (0)
#define STAMPP(i) do { } while (0)
#define STAMPG(i) do { } while (0)
#define STAMPE(i) STAMP(i)
#define VALIDATE(N, REF, RELOAD) VALIDATES(N, REF, RELOAD, VSITE)
#define VALIDATES(N, REF, RELOAD, SITE)                                              \
  do {                                                                        \
    unsigned m_ = 0;                                                          \
    _Pragma("unroll") for (int i_ = 0; i_ < (N); ++i_) m_ = mx4(m_, REF(i_)); \
    if (__builtin_expect(__any(m_ == POISON), 0)) {                           \
      _Pragma("unroll 1") for (int sp_ = 0;; ++sp_) {                         \
        if (dead || sp_ > SPIN_CAP) { dead = 1; break; }                      \
        __builtin_amdgcn_s_sleep(1); RETRY(SITE);                             \
        asm volatile("" ::: "memory");                                        \
        _Pragma("unroll") for (int i_ = 0; i_ < (N); ++i_) RELOAD(i_);        \
        unsigned n_ = 0;                                                      \
        _Pragma("unroll") for (int i_ = 0; i_ < (N); ++i_) n_ = mx4(n_, REF(i_)); \
        if (!__any(n_ == POISON)) break;                                      \
      }                                                                       \
    }                                                                         \
  } while (0)

template <int V> struct IC { static constexpr int value = V; constexpr operator int() const { return V; } };
template <int B, int E, class F> DI void sfor(F&& f) { if constexpr (B < E) { f(IC<B>{}); sfor<B + 1, E>(f); } }
struct Stats { float m, s, rs; };
DI unsigned lxpat(unsigned ln, unsigned rowb) { return ((ln >> 4) & 1) * rowb + (ln & 15) * 16 + (ln >> 5) * 256; }
DI unsigned lstpat(unsigned ln) { return (ln & 31) * 8 + (ln >> 5) * 256; }

__global__ __launch_bounds__(256, 1) void mega(Params p) {
  extern __shared__ __attribute__((aligned(16))) unsigned char smem[];
  LAS unsigned char* const L = (LAS unsigned char*)smem;
  const int tid = threadIdx.x, lane = tid & 63;
  const int b = blockIdx.x, idx = b >> 3;
  int w = __builtin_amdgcn_readfirstlane(tid >> 6), r = ((b & 7) >> 1) * 4 + (idx >> 3), c = (b & 1) * 8 + (idx & 7);
  const int hi = lane >> 5;
  const rsrc_t rs = __builtin_amdgcn_make_buffer_rsrc(p.ws, 0, WS_BYTES, 0x00020000);
  int dead = 0;
  u64 code_pc; unsigned code_len;
  asm volatile("s_getpc_b64 %0\n.Lmega_code_beg:\n\ts_mov_b32 %1, .Lmega_code_end-.Lmega_code_beg" : "=s"(code_pc), "=s"(code_len));
  if ((int)code_len < 0) code_len = 0;
  STAMP(0); STAMPC(62);

  const bool act = (c & 7) != 7;
  const int th = c >> 3;
  int cg = act ? (c & 7) : 0;
  int q = (b & 7) * 32 + idx;
  int T32a = 4 * r + w;
  int T32o = 4 * r + 2 * th + (w >> 1);
  const unsigned char* wsp = p.ws;
#define FRESH() asm volatile("" : "+s"(w), "+s"(c), "+s"(r), "+s"(cg), "+s"(q), "+s"(T32a), "+s"(T32o), "+s"(wsp))
#define S_XA(xb) (O_X + (xb) * XSZ + (unsigned)opqs(T32a) * (2 * 56 * 256))
#define S_STA(xb) (O_ST + (xb) * STSZ + (unsigned)opqs(T32a) * (14 * 256))
#define S_OFST(l) (O_OF + (l) * OFSZ + (unsigned)opqs(T32a) * (2 * 64 * 256) + (unsigned)opqs(c) * (4 * 256))
#define S_HBST(l) (O_HB + (l) * HBSZ + (unsigned)opqs(T32a) * (2 * 224 * 256) + (unsigned)opqs(c) * (14 * 256))
#define S_XST ((unsigned)opqs(T32o) * (2 * 56 * 256) + (unsigned)(8 * opqs(cg) + 4 * (w & 1)) * 256)
#define S_STST ((unsigned)(opqs(T32o) * 14 + 2 * opqs(cg) + (w & 1)) * 256)
#define S_KVP(l) (O_KVP + (l) * KVPSZ + (unsigned)(opqs(c) * 16 + opqs(r)) * 8192)
#define S_KVRST(l) (O_KVR + (l) * KVRSZ + (unsigned)(opqs(c) * 4096 + opqs(r) * 256))
  constexpr unsigned OOB = 0x80000000u;
  constexpr unsigned ONE2 = 0x3F803F80u;
#define P4 ([]() { v4u v_; asm volatile("v_mov_b32 %0, -1\n\tv_mov_b32 %1, -1\n\tv_mov_b32 %2, -1\n\tv_mov_b32 %3, -1" : "=v"(v_.x), "=v"(v_.y), "=v"(v_.z), "=v"(v_.w)); return v_; }())
#define P2 ([]() { v2u v_; asm volatile("v_mov_b32 %0, -1\n\tv_mov_b32 %1, -1" : "=v"(v_.x), "=v"(v_.y)); return v_; }())

  const v4u rqkv = mkrs(p.wqkv, 448 * 1344 * 2), rx = mkrs(p.x, 2048 * 448 * 2);
  const unsigned vwq = (unsigned)(lane >> 2) * 2688 + ((lane & 3) == 3 ? 40u : (unsigned)(lane & 3) * 16) + (unsigned)c * 56;
  const unsigned xsb = (unsigned)(56 + 24 * w + (w >> 1) * 8) * 1024;
  const unsigned vx0 = (unsigned)(lane >> 3) * 896 + (((unsigned)lane & 7) ^ (unsigned)(lane >> 3)) * 16;
  auto chunk = [&](int lc) {
    const unsigned sx = (unsigned)T32a * (32 * 896) + lc * 128, s = 4 * lc + w;
#pragma unroll
    for (int rg = 0; rg < 4; ++rg) dmab(rx, rg < 2 ? vx0 : vx0 ^ 16, sx + rg * 7168, xsb + ((lc % 6) * 4 + rg) * 1024);
    dmab(rqkv, vwq, s * 43008 + 896, s * 2048); dmab(rqkv, vwq, s * 43008 + 1792, s * 2048 + 1024);
  };
  chunk(0);
  if (tid < 8) *(LAS unsigned*)(L + L_SIG + tid * 4) = 0u;
  bar();
#pragma unroll
  for (int i = 1; i < LOOKA; ++i) chunk(i);
  unsigned tlo, thi;
  {
    const u64 ep = *(const __attribute__((address_space(4))) u64*)(p.ws + O_CTRL);
    const u64 tagb = (ep ^ 0x9E3779B97F4A7C15ull) << 8;
    tlo = (unsigned)tagb; thi = (unsigned)(tagb >> 32);
  }
  STAMP(1);

  auto prog = [&](unsigned v) { asm volatile("" ::: "memory"); *(volatile LAS unsigned*)(L + L_SIG + w * 4) = v; asm volatile("" ::: "memory"); };
  auto peek = [&]() -> v4u { asm volatile("" ::: "memory"); return *(volatile LAS v4u*)(L + L_SIG); };
  auto need = [&](v4u f, unsigned t) {
    if (__builtin_expect((unsigned)__builtin_amdgcn_readfirstlane(min(min(f.x, f.y), min(f.z, f.w))) < t, 0)) {
#pragma unroll 1
      for (int sp = 0;; ++sp) {
        const v4u g = peek();
        if ((unsigned)__builtin_amdgcn_readfirstlane(min(min(g.x, g.y), min(g.z, g.w))) >= t) break;
        if (sp > (SPIN_CAP << 6)) { dead = 1; break; }
      }
    }
    asm volatile("" ::: "memory");
  };
  auto await = [&](unsigned t) { need(peek(), t); };
  auto stage_set = [&](unsigned v) { asm volatile("" ::: "memory"); *(volatile LAS unsigned*)(L + L_SIG + 16) = v; asm volatile("" ::: "memory"); };
  auto stage_wait = [&](unsigned t) {
#pragma unroll 1
    for (int sp = 0;; ++sp) {
      asm volatile("" ::: "memory");
      if ((unsigned)__builtin_amdgcn_readfirstlane(*(volatile LAS unsigned*)(L + L_SIG + 16)) >= t) break;
      if (sp > (SPIN_CAP << 6)) { dead = 1; break; }
    }
    asm volatile("" ::: "memory");
  };
  auto publish = [&](unsigned stage) {
    const unsigned ln = opq(lane); stg2(v2u{tlo + stage, thi}, rs, ln < 8 ? ln * 2048 : OOB, O_FLG + (unsigned)opqs(q) * 8);
  };
  auto check = [&](unsigned stage) {
    const unsigned vo = opq(lane) * 32, so = O_FLG + (unsigned)(opqs(b) & 7) * 2048;
#pragma unroll 1
    for (int sp = 0;; ++sp) {
      asm volatile("" ::: "memory");
      const v4u a = ldg(rs, vo, so), d = ldg(rs, vo + 16, so);
      const bool ok = a.y == thi && a.w == thi && d.y == thi && d.w == thi && (a.x - tlo - stage) < 8u && (a.z - tlo - stage) < 8u &&
                      (d.x - tlo - stage) < 8u && (d.z - tlo - stage) < 8u;
      if (__all(ok)) break;
      if (dead || sp > SPIN_CAP) { dead = 1; break; }
      __builtin_amdgcn_s_sleep(4);
    }
  };
  auto poison_of = [&](int l, int i) { stg(P4, rs, lxpat(opq(lane), 64 * 256) + i * 512, S_OFST(l)); };
  auto poison_x = [&](int xb, int i) {
    const unsigned ln = opq(lane);
    if (i < 2) stg(P4, rs, act ? lxpat(ln, 56 * 256) + i * 512 : OOB, O_X + xb * XSZ + S_XST);
    else stg2(P2, rs, (act && ln < 32) ? ln * 8 : OOB, O_ST + xb * STSZ + S_STST);
  };
  auto poison_h = [&](int l, int i) { stg(P4, rs, lxpat(opq(lane), 224 * 256) + i * 512, S_HBST(l)); };

  auto split2 = [&](float x, float y) -> v4u {
    const float x0 = bfr(x), x1 = bfr(x - x0), x2 = (x - x0) - x1;
    const float y0 = bfr(y), y1 = bfr(y - y0), y2 = (y - y0) - y1;
    return v4u{pk(x0, x1), pk(x2, y0), pk(y1, y2), 0u};
  };
  auto mk_stats = [&](float mean, float var) -> Stats {
    Stats o; o.m = bfr(mean);
    o.s = bfr(__builtin_amdgcn_sqrtf(bfr(bfr(var) + 1e-5f)));
    o.rs = rcp(o.s);
    return o;
  };
#define PIN(x) asm volatile("" : "+v"(x))
  float lt0, lt1, lt2;
  auto ln_step = [&](int k, v2u (&st)[7], Stats& o) {
    if (k == 0) { lt0 = ((u2f(st[0].x) + u2f(st[1].x)) + (u2f(st[2].x) + u2f(st[3].x))) + ((u2f(st[4].x) + u2f(st[5].x)) + u2f(st[6].x)); PIN(lt0); }
    if (k == 1) { lt0 = hsum(lt0) * (1.0f / 448.0f); PIN(lt0);
                  lt1 = ((u2f(st[0].y) + u2f(st[1].y)) + (u2f(st[2].y) + u2f(st[3].y))); PIN(lt1); }
    if (k == 2) { lt1 += ((u2f(st[4].y) + u2f(st[5].y)) + u2f(st[6].y)); PIN(lt1);
                  const float d0 = u2f(st[0].x) * (1.0f / 32.0f) - lt0, d1 = u2f(st[1].x) * (1.0f / 32.0f) - lt0; lt2 = d0 * d0; lt2 += d1 * d1; PIN(lt2); }
    if (k == 3) { _Pragma("unroll") for (int i = 2; i < 5; ++i) { const float d = u2f(st[i].x) * (1.0f / 32.0f) - lt0; lt2 += d * d; } PIN(lt2); }
    if (k == 4) { _Pragma("unroll") for (int i = 5; i < 7; ++i) { const float d = u2f(st[i].x) * (1.0f / 32.0f) - lt0; lt2 += d * d; } lt1 += 32.0f * lt2; PIN(lt1); o.m = bfr(lt0); PIN(o.m); }
    if (k == 5) { lt1 = bfr(hsum(lt1) * (1.0f / 448.0f)); PIN(lt1); }
    if (k == 6) { lt1 = __builtin_amdgcn_sqrtf(bfr(lt1 + 1e-5f)); PIN(lt1); }
    if (k == 7) { o.s = bfr(lt1); o.rs = rcp(o.s); PIN(o.s); PIN(o.rs); }
  };

  v4u wdn[20][2];
  v4u wph[2];
  v4u cfa, cfd[2];
  unsigned rsd[8];

  auto red_put = [&](const f32x16& t, int a, unsigned l16) {
    LAS f32x4* d = (LAS f32x4*)(L + L_SCR + ((w ^ a) * 3 + (a - 1)) * 4096 + l16);
#pragma unroll
    for (int qq = 0; qq < 4; ++qq) d[qq * 64] = f32x4{t[4 * qq], t[4 * qq + 1], t[4 * qq + 2], t[4 * qq + 3]};
  };
  auto finish = [&](const f32x16& a0, int xb, bool last) {
    const unsigned ln = opq(lane), l16 = ln * 16;
    float res[16];
#pragma unroll
    for (int qq = 0; qq < 8; ++qq) { res[2 * qq] = blo(rsd[qq]); res[2 * qq + 1] = bhi(rsd[qq]); }
    bar();
    unsigned o[8];
    f32x4 t[4][3];
#pragma unroll
    for (int qq = 0; qq < 4; ++qq) {
      const LAS f32x4* s = (const LAS f32x4*)(L + L_SCR + (w * 3) * 4096 + l16) + qq * 64;
      t[qq][0] = s[0]; t[qq][1] = s[256]; t[qq][2] = s[512];
    }
#pragma unroll
    for (int i = 0; i < 16; ++i) res[i] += a0[i];
#pragma unroll
    for (int qq = 0; qq < 4; ++qq) {
#pragma unroll
      for (int i = 0; i < 4; ++i) res[4 * qq + i] = (res[4 * qq + i] + t[qq][0][i]) + (t[qq][1][i] + t[qq][2][i]);
      o[2 * qq] = san(pk(res[4 * qq], res[4 * qq + 1])); o[2 * qq + 1] = san(pk(res[4 * qq + 2], res[4 * qq + 3]));
    }
    const v4u p0 = widen(o[0], o[1], o[2], o[3]), p1 = widen(o[4], o[5], o[6], o[7]);
    if (last) {
      if (act) {
        GAS v4u* op = (GAS v4u*)(p.out + (size_t)(32 * T32o + (ln & 31)) * 448 + 64 * cg + 32 * (w & 1) + 8 * (ln >> 5));
        op[0] = p0; op[2] = p1;
      }
      return;
    }
    const unsigned so = act ? O_X + xb * XSZ : O_DUMX, so2 = act ? O_ST + xb * STSZ : O_DUMST;
    { const unsigned v = lxpat(ln, 56 * 256), s2 = so + S_XST; stg(p0, rs, v, s2); stg(p1, rs, v + 512, s2); }
    sb();
    float s1a = 0.f, s1b = 0.f;
#pragma unroll
    for (int qq = 0; qq < 8; ++qq) { rsd[qq] = o[qq]; res[2 * qq] = blo(o[qq]); res[2 * qq + 1] = bhi(o[qq]); s1a += res[2 * qq]; s1b += res[2 * qq + 1]; }
    const float s1 = hsum(s1a + s1b);
    const float mu = s1 * (1.0f / 32.0f);
    float m2a = 0.f, m2b = 0.f;
#pragma unroll
    for (int i = 0; i < 8; ++i) { const float d = res[2 * i] - mu, e = res[2 * i + 1] - mu; m2a += d * d; m2b += e * e; }
    const float m2 = hsum(m2a + m2b);
    stg2(v2u{san(f2u(s1)), san(f2u(m2))}, rs, ln < 32 ? ln * 8 : OOB, so2 + S_STST);
  };

  auto dual_softmax = [&](const f32x16& a, float sc, float osc, unsigned (&o)[16], auto&& hook) {
    float mx = fmaxf(fmaxf(a[0], a[1]), a[2]), mn = fminf(fminf(a[0], a[1]), a[2]);
    float mxb = fmaxf(fmaxf(a[3], a[4]), a[5]), mnb = fminf(fminf(a[3], a[4]), a[5]);
    mx = fmaxf(fmaxf(mx, a[6]), a[7]); mn = fminf(fminf(mn, a[6]), a[7]);
    mxb = fmaxf(fmaxf(mxb, a[8]), a[9]); mnb = fminf(fminf(mnb, a[8]), a[9]);
    mx = fmaxf(fmaxf(mx, a[10]), a[11]); mn = fminf(fminf(mn, a[10]), a[11]);
    float mx2 = fmaxf(fmaxf(a[12], a[13]), fmaxf(a[14], a[15])), mn2 = fminf(fminf(a[12], a[13]), fminf(a[14], a[15]));
    if (hi) { mx2 = a[0]; mn2 = a[0]; }
    mx = hmax(fmaxf(fmaxf(mx, mxb), mx2)); mn = hmin(fminf(fminf(mn, mnb), mn2));
    const float k = sc * 1.4426950408889634f;
    const float cx = -mx * k, cn = mn * k;
    float ep[16], en[16];
    hook(IC<0>{});
    sfor<0, 4>([&](auto G) {
      constexpr int g = decltype(G)::value;
#pragma unroll
      for (int i = 4 * g; i < 4 * g + 4; ++i) {
        ep[i] = __builtin_amdgcn_exp2f(__builtin_fmaf(a[i], k, cx));
        en[i] = __builtin_amdgcn_exp2f(__builtin_fmaf(a[i], -k, cn));
        if (i >= 12 && hi) { ep[i] = 0.f; en[i] = 0.f; }
      }
      hook(IC<1 + g>{});
    });
    float sp[4], sn[4];
#pragma unroll
    for (int i = 0; i < 4; ++i) { sp[i] = (ep[i] + ep[4 + i]) + (ep[8 + i] + ep[12 + i]); sn[i] = (en[i] + en[4 + i]) + (en[8 + i] + en[12 + i]); }
    const float rp = osc * rcp(hsum((sp[0] + sp[1]) + (sp[2] + sp[3]))), rn = osc * rcp(hsum((sn[0] + sn[1]) + (sn[2] + sn[3])));
    hook(IC<5>{});
#pragma unroll
    for (int i = 0; i < 8; ++i) o[i] = pk(ep[2 * i] * rp, ep[2 * i + 1] * rp);
    hook(IC<6>{});
#pragma unroll
    for (int i = 0; i < 8; ++i) o[8 + i] = pk(en[2 * i] * rn, en[2 * i + 1] * rn);
  };
  auto trbase = [&]() -> unsigned { const unsigned ln = opq(lane), G = ln >> 4; return (8 * (G >> 1) + ((ln & 15) >> 2)) * 64 + (G & 1) * 32 + (ln & 3) * 8; };
  auto dma_tile = [&](int t, int kb) {
    const unsigned char* g = wsp + O_GUT + (opqs(c) * 7 + t) * 28 * 1024 + w * 7168;
    const unsigned l16 = opq(lane) * 16, ld = (unsigned)(kb * 1024 + w * 7168);
    dmak<4>(g, ld, l16);
    if (t == 3) { dmak<2>(g + 4096, ld + 4096, l16); if (w < 3) dmak<1>(g + 6144, ld + 6144, l16); }
    else dmak<3>(g + 4096, ld + 4096, l16);
  };

  v4u xf[28];
  f32x16 ak = {}, av = {};
  Stats sa;
  {
    unsigned tb[4], xa[4];
    { const unsigned t0 = trbase(), ln = opq(lane), tk = ln & 31, rg = tk >> 3, r8 = tk & 7;
      const unsigned a0 = xsb + rg * 1024 + r8 * 128, sw = (ln >> 5) ^ r8 ^ (rg >> 1) ^ (2 * w);
#pragma unroll
      for (int j = 0; j < 4; ++j) { tb[j] = t0 + (unsigned)(j ^ w) * 2048; xa[j] = a0 + (sw ^ (2 * j)) * 16; } }
    const unsigned zj = (unsigned)w * 2048 + 1024 + (unsigned)(lane & 15) * 64 + 48;
    f32x16 cs = {};
    float s1[2] = {0.f, 0.f}, s2[2] = {0.f, 0.f};
    const unsigned z0 = (lane & 31) == 0 ? ONE2 : 0u, z4 = (lane & 31) == 4 ? ONE2 : 0u;
    unsigned e[2][8]; v2u rv[4];
    sfor<0, 7>([&](auto N) {
      constexpr int i = decltype(N)::value;
      constexpr int last = i + LOOKA - 1 < 6 ? i + LOOKA - 1 : 6;
      vmwait<6 * (last - i) + (i + LOOKA - 1 > 6 ? 28 : 0)>();
      STAMPP(53 + i);
      *(LAS v2u*)(L + zj + i * 8192) = v2u{0u, 0u};
      prog(i + 1);
#pragma unroll
      for (int j = 0; j < 4; ++j) xf[4 * i + j] = *(const LAS v4u*)(L + xa[j] + (i % 6) * 4096);
      if constexpr (i + LOOKA < 7) chunk(i + LOOKA);
      if constexpr (i + LOOKA == 7) {
        const unsigned ln = opq(lane), l16 = ln * 16, tk = ln & 31, h2 = ln >> 5;
        stg(P4, rs, l16, S_KVP(0) + w * 2048); stg(P4, rs, l16 + 1024, S_KVP(0) + w * 2048);
        poison_of(0, 0); poison_of(0, 1);
        stg(P4, rs, (w == 2 && ln < 16) ? l16 : OOB, S_KVRST(0));
        stg(P4, rs, l16, S_KVP(1) + w * 2048); stg(P4, rs, l16 + 1024, S_KVP(1) + w * 2048);
        stg(P4, rs, (w == 2 && ln < 16) ? l16 : OOB, S_KVRST(1));
        const rsrc_t rp = __builtin_amdgcn_make_buffer_rsrc((void*)(p.wphi + c * 784), 0, 784 * 2, 0x00020000);
#pragma unroll
        for (int g = 0; g < 4; ++g) {
          const unsigned vo = g < 3 ? (tk < 28 ? (tk + 4 * h2 * 28) * 2 : OOB) : ((tk < 28 && h2) ? tk * 2 : OOB);
#pragma unroll
          for (int j = 0; j < 4; ++j) e[g >> 1][4 * (g & 1) + j] = (unsigned short)__builtin_amdgcn_raw_buffer_load_b16(rp, vo, (8 * g + j) * 56, 0);
        }
        const GAS v2u* xp = (const GAS v2u*)(p.x + (size_t)(32 * T32o + tk) * 448 + 64 * cg + 32 * (w & 1) + 4 * h2);
#pragma unroll
        for (int qq = 0; qq < 4; ++qq) rv[qq] = xp[2 * qq];
      }
      sb();
      await(i + 1);
      v4u wk[4], wv[4];
#pragma unroll
      for (int j = 0; j < 4; ++j) { wk[j] = trf(L + tb[j] + i * 8192, 256); wv[j] = trf(L + tb[j] + i * 8192 + 1024, 256); }
      sb();
#pragma unroll
      for (int j = 0; j < 4; ++j) {
        const v4u xv = xf[4 * i + j];
        s1[0] = dot2(xv.x, ONE2, s1[0]); s1[1] = dot2(xv.y, ONE2, s1[1]); s2[0] = dot2(xv.x, xv.x, s2[0]); s2[1] = dot2(xv.y, xv.y, s2[1]);
        ak = mfma(wk[j], xv, ak);
        s1[0] = dot2(xv.z, ONE2, s1[0]); s1[1] = dot2(xv.w, ONE2, s1[1]); s2[0] = dot2(xv.z, xv.z, s2[0]); s2[1] = dot2(xv.w, xv.w, s2[1]);
        av = mfma(xv, wv[j], av);
        if (j == 0) { cs = mfma(v4u{z0, z0, z0, z0}, wk[0], cs); cs = mfma(v4u{z4, z4, z4, z4}, wv[0], cs); }
        sb();
      }
    });
    STAMPP(60);
    *(LAS float*)(L + L_SUM + w * 256 + lane * 4) = cs[0];
    const float mean = hsum(s1[0] + s1[1]) * (1.0f / 448.0f);
    sa = mk_stats(mean, fmaxf(hsum(s2[0] + s2[1]) * (1.0f / 448.0f) - mean * mean, 0.f));
#pragma unroll
    for (int s = 0; s < 2; ++s) wph[s] = v4u{e[s][0] | (e[s][1] << 16), e[s][2] | (e[s][3] << 16), e[s][4] | (e[s][5] << 16), e[s][6] | (e[s][7] << 16)};
#pragma unroll
    for (int qq = 0; qq < 4; ++qq) { rsd[2 * qq] = rv[qq].x; rsd[2 * qq + 1] = rv[qq].y; }
    drain();
    bar();
    if (w == 3) publish(1);
    const LAS float* sp = (const LAS float*)(L + L_SUM + lane * 4);
    const float cskv = (sp[0] + sp[64]) + (sp[128] + sp[192]);
    cfa = hi ? split2(cskv, (lane & 31) == 24 ? 1.0f : 0.0f) : split2(0.f, cskv);
  }

#define LDSF(kb) (*(const LAS v4u*)(L + ((kb) < 64 ? lane16 + (kb) * 1024 : (kb) < 128 ? lane16b + ((kb) - 64) * 1024 : lane16c + ((kb) - 128) * 1024)))
#define GEMM2(RD, M0, M1, STATS)                                                                          \
  do {                                                                                                    \
    v4u wb[2][2]; v2u st[7]; unsigned mm = 0;                                                             \
    _Pragma("unroll") for (int s = 0; s < 12; ++s) XLD(s);                                                \
    sb();                                                                                                 \
    PRE_;                                                                                                 \
    _Pragma("unroll") for (int s = 0; s < 2; ++s) { wb[s][0] = RD(s, 0); wb[s][1] = RD(s, 1); }           \
    _Pragma("unroll") for (int g = 0; g < 7; ++g) {                                                       \
      if (g < 4) { _Pragma("unroll") for (int i = 0; i < 4; ++i) mm = mx4(mm, xf[4 * g + i]); }           \
      if (__builtin_expect(__any(mm == POISON), 0)) {     \
        VALIDATE(4, XREF_, XREL_);                                                                        \
        _Pragma("unroll") for (int s = 4 * g + 4; s < 28 && s < 12 + 8 * g; ++s) XLD(s);                  \
      }                                                                                                   \
      mm = 0;                                                                                             \
      if (g == 5) {                                                                                       \
        unsigned m_ = 0;                                                                                  \
        _Pragma("unroll") for (int i = 0; i < 7; ++i) m_ = umax(umax(m_, st[i].x), st[i].y);              \
        if (__builtin_expect(__any(m_ == POISON), 0)) {                                                   \
          _Pragma("unroll 1") for (int sp = 0;; ++sp) {                                                   \
            if (dead || sp > SPIN_CAP) { dead = 1; break; }                                               \
            RETRY(52);                                                                                    \
            asm volatile("" ::: "memory");                                                                \
            unsigned n_ = 0;                                                                              \
            _Pragma("unroll") for (int i = 0; i < 7; ++i) st[i] = ldg2(rs, vst + i * 512, so2);           \
            _Pragma("unroll") for (int i = 0; i < 7; ++i) n_ = umax(umax(n_, st[i].x), st[i].y);          \
            if (!__any(n_ == POISON)) break;                                                              \
          }                                                                                               \
        }                                                                                                 \
      }                                                                                                   \
      STAMPG(53 + g);                                                                                     \
      _Pragma("unroll") for (int i = 0; i < 4; ++i) {                                                     \
        const int s = 4 * g + i;                                                                          \
        M0(wb[s & 1][0], xf[s]);                                                                          \
        if (s + 2 < 28) wb[s & 1][0] = RD(s + 2, 0);                                                      \
        if (2 * s + 12 < 28) XLD(2 * s + 12);                                                             \
        M1(wb[s & 1][1], xf[s]);                                                                          \
        if (s + 2 < 28) wb[s & 1][1] = RD(s + 2, 1);                                                      \
        if (2 * s + 13 < 28) XLD(2 * s + 13);                                                             \
        if (g >= 3 && g < 6) mm = mx4(mm, xf[s + 4]);                                                     \
        if (s >= 8 && s < 12) { st[2 * (s - 8)] = ldg2(rs, vst + 2 * (s - 8) * 512, so2); if (s < 11) st[2 * (s - 8) + 1] = ldg2(rs, vst + (2 * (s - 8) + 1) * 512, so2); } \
        if (s >= 20) ln_step(s - 20, st, STATS);                                                          \
        HOOK_(s);                                                                                         \
        if (i < 3 || g >= 3) sb();                                                                        \
      }                                                                                                   \
    }                                                                                                     \
  } while (0)
#define XREF_(i) xf[4 * g + (i)]
#define XREL_(i) XLD(4 * g + (i))

#pragma unroll
  for (int i = 0; i < 20; ++i) asm volatile("" : "=a"(wdn[i][0]), "=a"(wdn[i][1]));
  asm volatile("" : "=v"(cfd[0]), "=v"(cfd[1]));
  v4u wf[8][2];
#pragma unroll
  for (int ks = 0; ks < 8; ++ks) asm volatile("" : "=v"(wf[ks][0]), "=v"(wf[ks][1]));
#pragma unroll 1
  for (int l = 0;; l = 1) {
    const unsigned lane16 = opq(lane) * 16;
    const unsigned lane16b = opq(lane16 + 65536), lane16c = opq(lane16 + 131072);
    STAMP(4 + 16 * l);
    FRESH();
    const unsigned nm2 = pk(-sa.m, -sa.m);
#define WOLD do { const unsigned swo = O_WOT + (unsigned)(opqs(cg) * 4 + w) * 16384;                       \
      _Pragma("unroll") for (int ks = 0; ks < 8; ++ks) _Pragma("unroll") for (int n = 0; n < 2; ++n)      \
        wf[ks][n] = ldg(rs, lane16 + (ks & 1) * 2048, swo + (unsigned)(n ^ (w & 1)) * 1024 + (ks >> 1) * 4096); sb(); } while (0)
    {
      LAS unsigned char* const SW = L + L_SCR + w * 6400;
      ak = mfma(cfa, hi ? v4u{0u, 0u, 0u, 0u} : v4u{0u, nm2 & 0xFFFF0000u, nm2, 0u}, ak);
      av = mfma(hi ? v4u{nm2, pk(-sa.m, sa.s), 0u, 0u} : v4u{0u, 0u, 0u, 0u}, cfa, av);
      f32x16 pkk = {};
      pkk = mfma(wph[0], v4u{pk(ak[0], ak[1]), pk(ak[2], ak[3]), pk(ak[4], ak[5]), pk(ak[6], ak[7])}, pkk);
      pkk = mfma(wph[1], v4u{pk(ak[8], ak[9]), pk(ak[10], ak[11]), pk(ak[12], ak[13]), pk(ak[14], ak[15])}, pkk);
      *(LAS v4u*)(SW + 4352 + lane16) = v4u{pk(av[0], av[1]), pk(av[2], av[3]), pk(av[4], av[5]), pk(av[6], av[7])};
      *(LAS v4u*)(SW + 5376 + lane16) = v4u{pk(av[8], av[9]), pk(av[10], av[11]), pk(av[12], av[13]), pk(av[14], av[15])};
      unsigned ph[16];
      dual_softmax(pkk, sa.rs, sa.rs, ph, [&](auto) {});
      const unsigned ln2 = opq(lane), phw = (ln2 & 31) * 136 + (ln2 >> 5) * 8;
#pragma unroll
      for (int t = 0; t < 2; ++t)
#pragma unroll
        for (int qq = 0; qq < 4; ++qq) *(LAS v2u*)(SW + phw + t * 64 + qq * 16) = v2u{ph[8 * t + 2 * qq], ph[8 * t + 2 * qq + 1]};
      bar();
      STAMP(34 + l);
      if (w < 2) {
        const unsigned ln = opq(lane), G = ln >> 4, q4 = (ln & 15) >> 2, p4 = ln & 3;
        const LAS unsigned char* tb = L + L_SCR + (4 * (G >> 1) + q4) * 136 + (16 * (G & 1) + 4 * p4) * 2 + w * 64;
        v4u fa[8], fb[8];
#pragma unroll
        for (int i = 0; i < 8; ++i) {
          const int ww = i >> 1, s = i & 1;
          fa[i] = trf(tb + ww * 6400 + (16 * s) * 136, 8 * 136);
          fb[i] = *(const LAS v4u*)(L + L_SCR + ww * 6400 + 4352 + s * 1024 + lane16);
        }
        f32x16 kv = {};
#pragma unroll
        for (int i = 0; i < 8; ++i) kv = mfma(fa[i], fb[i], kv);
        const unsigned so = S_KVP(l) + w * 4096;
#pragma unroll
        for (int qq = 0; qq < 4; ++qq)
          stg(v4u{san(f2u(kv[4 * qq])), san(f2u(kv[4 * qq + 1])), san(f2u(kv[4 * qq + 2])), san(f2u(kv[4 * qq + 3]))}, rs, lane16 + qq * 1024, so);
      }
    }
    STAMP(5 + 16 * l);
    FRESH();
    if (l == 0) {
      const bool jgu = q < 112, has_job = jgu || w >= 2;
      const int jj = has_job ? (q - 112) * 2 + (w - 2) : 0;
      const unsigned jsb = (unsigned)(w < 3 ? 84 + 8 * w : 140) * 1024;
      if (w >= 2) {
#pragma unroll 1
        for (int i = 0; i < 14; ++i) { const unsigned ks = 2 * i + (w & 1); dmab(rqkv, vwq, ks * 43008, (56 + ks) * 1024); }
      }
      v4u rj0, rj1; unsigned jso0, jso1, jld;
      if (jgu) { const int cc = q / 7, s = 4 * (q - 7 * cc) + w; rj0 = mkrs(p.wg, 448 * 1792 * 2); rj1 = mkrs(p.wu, 448 * 1792 * 2); jld = 3584; jso0 = jso1 = s * 16 * 3584 + cc * 224; }
      else if (jj < 224) { rj0 = rj1 = mkrs(p.wd, 1792 * 448 * 2); jld = 896; jso0 = (jj >> 1) * 16 * 896 + (jj & 1) * 448; jso1 = jso0 + 224; }
      else { const int hp = (jj - 224) >> 1; rj0 = rj1 = mkrs(p.wo, 448 * 448 * 2); jld = 896; jso0 = (28 * (hp >> 1) + 16 * (hp & 1)) * 896 + (jj & 1) * 448; jso1 = jso0 + 224; }
      const unsigned ln = opq(lane), G = ln >> 4;
      if (has_job) {
#pragma unroll
        for (int i = 0; i < 4; ++i) {
          const unsigned pc = i * 64 + ln, row = pc / 14, vo = pc < 224 ? row * jld + (pc - row * 14) * 16 : OOB;
          dmab(rj0, vo, jso0, jsb + i * 1024); dmab(rj1, vo, jso1, jsb + 4096 + i * 1024);
        }
      }
      {
        const unsigned co = (unsigned)(idx * 4 + w) * 1024 + lane16;
        const v4u cpf = *(const GAS v4u*)(code_pc + (co + 16 <= code_len ? co : 0u));
        asm volatile("" :: "v"(cpf));
      }
      drain();
      prog(P_QW);
      STAMP(44);
      if (w == 3) { check(1); stage_set(1); }
      const unsigned jb = jsb + (8 * (G >> 1) + ((ln & 15) >> 2)) * 224 + (ln & 3) * 8;
      unsigned dst, du1, du2;
      int ct0 = 7 * (jj & 1); bool wcut = false;
      if (jgu) { const int cc = q / 7, s = 4 * (q - 7 * cc) + w; dst = O_GUT + (cc * 196 + s) * 1024; du1 = 28 * 1024; du2 = 56 * 1024; ct0 = 0; }
      else if (jj < 224) { const int ks = jj >> 1, cc = ks / 7, pp = ks - 7 * cc; dst = O_DNT + ((cc >> 2) * 56 + pp * 8 + (cc & 3) * 2) * 1024; du1 = 1024; du2 = 224 * 1024; }
      else { const int hp = (jj - 224) >> 1; dst = O_WOT + hp * 2048; du1 = 1024; du2 = 65536; wcut = (hp & 1) && hi; }
      if (!has_job) dst = OOB;
      f32x16 cs = {};
#pragma unroll
      for (int u = 0; u < 7; ++u) {
        const unsigned e = jgu ? u + 7 * (G & 1) : 2 * u + (G & 1);
        v4u fr = trf(L + jb + e * 32 + (e >= 7 ? 4096 - 224 : 0), 4 * 224);
        if (wcut) fr = v4u{0u, 0u, fr.x, fr.y};
        const int ct = ct0 + u;
        stg(fr, rs, lane16, dst + (ct >> 1) * du2 + (ct & 1) * du1);
        const unsigned z = (ln & 31) == (unsigned)u ? ONE2 : 0u;
        cs = mfma(v4u{z, z, z, z}, fr, cs);
      }
      await(P_QW);
#pragma unroll
      for (int i = 0; i < 2; ++i) {
        const int f = r + 16 * (w + 4 * i);
        const v4u v = *(const LAS v4u*)(L + (f < 84 ? f : 0) * 1024 + lane16);
        stg(v, rs, lane16, f < 84 ? O_QKVI + (c * 84 + f) * 1024 : OOB);
      }
      asm volatile("s_nop 15\n\ts_nop 7" ::: "memory");
#pragma unroll
      for (int i = 0; i < 4; ++i) *(LAS float*)(L + L_SUM + 2048 + w * 1024 + ((4 * (ln >> 5) + i) * 32 + (ln & 31)) * 4) = cs[i];
    } else if (w == 3) {
      const unsigned char* g = wsp + O_GUT + (opqs(c) * 7) * 28 * 1024;
#pragma unroll 1
      for (int i = 0; i < 14; ++i) dmak<4>(g + i * 4096, (unsigned)i * 4096, lane16);
    }
    STAMP(36 + l);
    FRESH();
    unsigned pq[16];
    {
      unsigned tb[4];
      { const unsigned t0 = trbase() + 56 * 1024;
#pragma unroll
        for (int j = 0; j < 4; ++j) tb[j] = t0 + (unsigned)(j ^ w) * 1024; }
#define QF(i) trf(L + tb[(i) & 3] + ((i) >> 2) * 4096, 256)
      v4u wb[4];
#pragma unroll
      for (int s = 0; s < 4; ++s) wb[s] = QF(s);
      f32x16 aq = {}, cs = {};
      const unsigned z0 = (lane16 & 511) == 0 ? ONE2 : 0u;
      sfor<0, 28>([&](auto N) {
        constexpr int s = decltype(N)::value;
        aq = mfma(wb[s & 3], xf[s], aq);
        if constexpr ((s & 3) == 0) cs = mfma(v4u{z0, z0, z0, z0}, wb[s & 3], cs);
        if constexpr (s + 4 < 28) wb[s & 3] = QF(s + 4);
        sb();
      });
#undef QF
      asm volatile("s_nop 15\n\ts_nop 7" ::: "memory");
      if (l == 0) {
        *(LAS float*)(L + L_SUM + 1024 + w * 256 + opq(lane) * 4) = cs[0];
        drain();
        prog(P_J);
      }
      STAMP(6 + 16 * l);
      FRESH();
      const int fr = r >> 2, lq = r & 3;
      const unsigned ln = opq(lane), vo = (ln >> 4) * 8192 + (ln & 15) * 16;
      v4u pa[8];
      const unsigned so = O_KVP + l * KVPSZ + (unsigned)(c * 128 + 2 * fr) * 1024 + (unsigned)lq * 256;
#define REF_(i) pa[i]
#define REL_(i) pa[i] = ldg(rs, vo + ((i) & 1) * 1024, so + ((i) >> 1) * 32768)
      if (w == 2) {
        if (l == 0) stage_wait(1);
#pragma unroll
        for (int i = 0; i < 8; ++i) REL_(i);
      } else {
#pragma unroll
        for (int i = 0; i < 8; ++i) asm volatile("" : "=v"(pa[i]));
      }
      poison_h(l, 0); poison_x(2 * l + 1, 0); poison_x(2 * l + 1, 1); poison_x(2 * l + 1, 2);
      if (w == 2) {
#define VSITE 46
        VALIDATE(8, REF_, REL_);
#undef VSITE
#undef REF_
#undef REL_
        float sv[8];
#pragma unroll
        for (int z = 0; z < 2; ++z)
#pragma unroll
          for (int i = 0; i < 4; ++i) {
            float t = (u2f(pa[z][i]) + u2f(pa[2 + z][i])) + (u2f(pa[4 + z][i]) + u2f(pa[6 + z][i]));
            auto r16 = __builtin_amdgcn_permlane16_swap(f2u(t), f2u(t), false, false);
            t = u2f(r16[0]) + u2f(r16[1]);
            sv[4 * z + i] = hsum(t);
          }
        const v4u o = {san(pk(sv[0], sv[1])), san(pk(sv[2], sv[3])), san(pk(sv[4], sv[5])), san(pk(sv[6], sv[7]))};
        stg(o, rs, ln < 16 ? ln * 16 : OOB, S_KVRST(l));
      }
      STAMP(15 + 16 * l);
      if (l == 0) {
        await(P_J);
        if (w == 3) {
          const int cc = q / 7, kq = q - 7 * cc;
#pragma unroll
          for (int k = 0; k < 4; ++k) {
            const unsigned ix = opq(lane) + 64 * k;
            const LAS float* sp = (const LAS float*)(L + L_SUM + 2048 + ix * 4);
            const float v = (sp[0] + sp[256]) + (sp[512] + sp[768]);
            __builtin_amdgcn_raw_buffer_store_b32(f2u(v), rs, (q < 112 && ix < 224) ? ix * 32 : OOB, O_CSG + cc * 7168 + kq * 4, 16);
          }
          drain();
          publish(2);
        }
        const LAS float* sp = (const LAS float*)(L + L_SUM + (opq(lane) & 31) * 4);
        const float cq = (sp[256] + sp[320]) + (sp[384] + sp[448]), ck = (sp[0] + sp[64]) + (sp[128] + sp[192]);
        if (!hi) cfa = split2(cq, ck);
      }
      aq = mfma(cfa, hi ? v4u{0u, 0u, 0u, 0u} : v4u{nm2, nm2 & 0xFFFFu, 0u, 0u}, aq);
      f32x16 pp = {};
      pp = mfma(wph[0], v4u{pk(aq[0], aq[1]), pk(aq[2], aq[3]), pk(aq[4], aq[5]), pk(aq[6], aq[7])}, pp);
      pp = mfma(wph[1], v4u{pk(aq[8], aq[9]), pk(aq[10], aq[11]), pk(aq[12], aq[13]), pk(aq[14], aq[15])}, pp);
      dual_softmax(pp, sa.rs, 1.0f, pq, [&](auto) {});
#pragma unroll
      for (int i = 0; i < 16; ++i) asm volatile("" : "+v"(pq[i]));
      STAMP(38 + l);
      drain();
    }
    bar();
    STAMP(7 + 16 * l);
    FRESH();
    {
      v4u kf[4];
      const unsigned so = O_KVR + l * KVRSZ + (unsigned)opqs(c) * 4096;
#define REF_(i) kf[i]
#define REL_(i) kf[i] = ldg(rs, lane16 + (i) * 1024, so)
#pragma unroll
      for (int i = 0; i < 4; ++i) REL_(i);
#define VSITE 47
      VALIDATE(4, REF_, REL_);
#undef VSITE
#undef REF_
#undef REL_
      f32x16 nm = {};
#pragma unroll
      for (int i = 0; i < 4; ++i) nm = mfma(kf[i], v4u{pq[4 * i], pq[4 * i + 1], pq[4 * i + 2], pq[4 * i + 3]}, nm);
      auto rd = __builtin_amdgcn_permlane32_swap(f2u(nm[12]), f2u(nm[12]), false, false);
      const float den = bfr(bfr(u2f(rd[0])) + 1e-6f);
      const float rden = rcp(den);
      unsigned o[8];
#pragma unroll
      for (int i = 0; i < 8; ++i) o[i] = san(pk(nm[2 * i] * rden, nm[2 * i + 1] * rden));
      if (!hi) { o[6] = 0u; o[7] = 0u; }
      const unsigned v64 = lxpat(opq(lane), 64 * 256), s2 = S_OFST(l);
      stg(widen(o[0], o[1], o[2], o[3]), rs, v64, s2);
      stg(widen(o[4], o[5], o[6], o[7]), rs, v64 + 512, s2);
    }
    STAMP(8 + 16 * l);
    FRESH();
    if (l) dma_tile(2, 56);
    else {
      poison_x(2, 0); poison_x(2, 1); poison_x(2, 2);
      if (w == 3) { check(2); stage_set(2); }
      stage_wait(2);
      WOLD;
      dma_tile(0, 0); dma_tile(1, 28);
      const unsigned ln = opq(lane), vg = (ln & 31) * 32 + (ln >> 5) * 2048;
      v4u cd[4][2];
#pragma unroll
      for (int i = 0; i < 4; ++i)
#pragma unroll
        for (int k = 0; k < 2; ++k) cd[i][k] = ldg(rs, vg + (i & 1) * 1024 + k * 16, O_CSG + c * 7168 + (i >> 1) * 4096);
      sb();
      float sm[4];
#pragma unroll
      for (int i = 0; i < 4; ++i)
        sm[i] = ((u2f(cd[i][0].x) + u2f(cd[i][0].y)) + (u2f(cd[i][0].z) + u2f(cd[i][0].w))) + ((u2f(cd[i][1].x) + u2f(cd[i][1].y)) + u2f(cd[i][1].z));
      cfd[0] = split2(sm[0], sm[1]);
      cfd[1] = split2(sm[2], hi ? 0.0f : sm[3]);
    }
    drain();
    {
      v4u of[8][2];
      const unsigned vo = lxpat(opq(lane), 64 * 256);
      const unsigned so0 = O_OF + l * OFSZ + (unsigned)(16 * w) * 256;
      const unsigned so[2] = {so0 + (unsigned)opqs(T32o) * (2 * 64 * 256), so0 + (unsigned)(opqs(T32o) ^ 1) * (2 * 64 * 256)};
#define KS_(i) (2 * (((i) >> 1) & 3) + ((i) < 8 ? 1 : 0))
#define OLD(i) of[KS_(i)][(i) & 1] = ldg(rs, vo + KS_(i) * 512, so[(i) & 1])
#pragma unroll
      for (int i = 0; i < 16; ++i) OLD(i);
      sb();
#pragma unroll
      for (int i = 1; i < 7; ++i) poison_h(l, i);
      sb();
      f32x16 acc[4] = {};
      unsigned mm = 0;
#pragma unroll
      for (int i = 0; i < 8; ++i) mm = mx4(mm, of[KS_(i)][i & 1]);
#define VSITE 48
      if (__builtin_expect(__any(mm == POISON), 0)) {
#define REF_(i) of[KS_(i)][(i) & 1]
#define REL_(i) OLD(i)
        VALIDATE(8, REF_, REL_);
#undef REF_
#undef REL_
#pragma unroll
        for (int i = 8; i < 16; ++i) OLD(i);
      }
      mm = 0;
#pragma unroll
      for (int i = 0; i < 16; ++i) {
        const int a = i & 3, ks = 2 * (i >> 2) + 1;
        acc[a] = mfma(wf[ks][a & 1], of[ks][a >> 1], acc[a]);
        if (i >= 8) mm = mx4(mm, of[KS_(i)][i & 1]);
        sb();
      }
      if (__builtin_expect(__any(mm == POISON), 0)) {
#define REF_(i) of[KS_(8 + (i))][(i) & 1]
#define REL_(i) OLD(8 + (i))
        VALIDATE(8, REF_, REL_);
#undef REF_
#undef REL_
      }
#undef VSITE
      if (l == 0) { dma_tile(2, 56); dma_tile(3, 84); }
#pragma unroll
      for (int aa = 1; aa < 5; ++aa) {
        const int a = aa & 3;
#pragma unroll
        for (int k2 = 0; k2 < 4; ++k2) acc[a] = mfma(wf[2 * k2][a & 1], of[2 * k2][a >> 1], acc[a]);
        if (aa > 1) red_put(acc[aa - 1], aa - 1, lane16);
        sb();
      }
#undef KS_
#undef OLD
      STAMP(9 + 16 * l);
      FRESH();
      finish(acc[0], 2 * l + 1, false);
    }
    STAMP(10 + 16 * l);
    FRESH();
    vmwait<1>();
    {
      const unsigned vh = lxpat(opq(lane), 224 * 256);
      const unsigned sh0 = O_HB + l * HBSZ + (unsigned)(56 * w) * 256;
      const unsigned sh[2] = {sh0 + (unsigned)opqs(T32o) * (2 * 224 * 256), sh0 + (unsigned)(opqs(T32o) ^ 1) * (2 * 224 * 256)};
      v4u hf[4][8];
#define HLOAD(g, i) hf[(g) % 4][i] = ldg(rs, vh + (g) * 512, sh[(i) & 1] + ((i) >> 1) * (14 * 256))
      STAMP(11 + 16 * l);
      f32x16 ac[7];
      ac[0] = f32x16{}; ac[1] = f32x16{};
      Stats sd;
      {
        const unsigned vx_a = lxpat(opq(lane), 56 * 256), vst = lstpat(opq(lane));
        const unsigned so = S_XA(2 * l + 1), so2 = S_STA(2 * l + 1);
        const unsigned char* g4 = wsp + O_GUT + (opqs(c) * 7 + 4) * 28 * 1024;
#define XLD(s) xf[s] = ldg(rs, vx_a + ((s) & 7) * 512, so + ((s) >> 3) * 4096)
#define RD_(s, t) LDSF(28 * (t) + (s))
#define M0_(wv, xv) ac[0] = mfma(wv, xv, ac[0])
#define M1_(wv, xv) ac[1] = mfma(wv, xv, ac[1])
#define VSITE 49
#define PRE_ rawbar()
#define HOOK_(s) do { if (s >= 20 && s < 27) dmak<1>(g4 + w * 7168 + (s - 20) * 1024, (unsigned)(112 * 1024 + w * 7168 + (s - 20) * 1024), lane16); \
                      if (s == 27) dmak<1>(g4 - 1024, (unsigned)((140 + w) * 1024), lane16); } while (0)
        GEMM2(RD_, M0_, M1_, sd);
#undef VSITE
#undef PRE_
#undef HOOK_
#undef RD_
#undef M0_
#undef M1_
#undef XLD
      }
      prog(P_G2 + 16 * l);
      STAMP(41);
      FRESH();
      const unsigned shb = S_HBST(l);
      const float nrs = -1.4426950408889634f * sd.rs;
      const unsigned dm2 = pk(-sd.m, -sd.m);
      auto corr = [&](int t) -> v4u {
        const bool on = ((t >> 1) & 1) == hi;
        const unsigned z = on ? dm2 : 0u;
        return (t & 1) ? v4u{0u, z & 0xFFFF0000u, z, 0u} : v4u{z, z & 0xFFFFu, 0u, 0u};
      };
#define FKB(n) ((n) == 55 ? 140 : (n) < 84 ? 56 + (n) : (n) - 84)
      v4u wb[5];
#pragma unroll
      for (int s = 0; s < 5; ++s) wb[s] = LDSF(FKB(s));
#pragma unroll
      for (int t = 0; t < 2; ++t) ac[t] = mfma(cfd[0], corr(t), ac[t]);
      const unsigned char* g56 = wsp + O_GUT + (opqs(c) * 7 + 5) * 28 * 1024 + w * 14336;
      const unsigned char* gq = wsp + O_QKVI + opqs(c) * 84 * 1024;
      const unsigned sdn = O_DNT + (unsigned)(opqs(cg) * 4 + w) * (56 * 1024);
      float tg[4], tu[4]; unsigned ho[4];
      auto epi_item = [&](auto T, auto K) {
        constexpr int t = decltype(T)::value, k = decltype(K)::value, e0 = k < 8 ? 0 : 4, kk = k & 7;
        if constexpr (k < 16) {
          if constexpr (kk == 0) { _Pragma("unroll") for (int i = 0; i < 4; ++i) { tg[i] = ac[t][e0 + i]; PIN(tg[i]); } }
          else if constexpr (kk == 1) { _Pragma("unroll") for (int i = 0; i < 4; ++i) { tg[i] *= nrs; PIN(tg[i]); } }
          else if constexpr (kk == 2 || kk == 3) { _Pragma("unroll") for (int i = 2 * (kk - 2); i < 2 * (kk - 2) + 2; ++i) { tg[i] = __builtin_amdgcn_exp2f(tg[i]); PIN(tg[i]); } tu[kk - 2] = ac[t][8 + e0 + kk - 2]; PIN(tu[kk - 2]); }
          else if constexpr (kk == 4) { _Pragma("unroll") for (int i = 0; i < 4; ++i) { tg[i] = __builtin_fmaf(tg[i], sd.s, sd.s); PIN(tg[i]); } }
          else if constexpr (kk == 5) { _Pragma("unroll") for (int i = 0; i < 2; ++i) { tg[i] = rcp(tg[i]); PIN(tg[i]); } tu[2] = ac[t][8 + e0 + 2]; PIN(tu[2]); }
          else if constexpr (kk == 6) { _Pragma("unroll") for (int i = 0; i < 2; ++i) { tg[2 + i] = rcp(tg[2 + i]); PIN(tg[2 + i]); } tu[3] = ac[t][8 + e0 + 3]; PIN(tu[3]); }
          else { ho[e0 >> 1] = pk(tu[0] * tg[0], tu[1] * tg[1]); ho[(e0 >> 1) + 1] = pk(tu[2] * tg[2], tu[3] * tg[3]); PIN(ho[e0 >> 1]); PIN(ho[(e0 >> 1) + 1]); }
        } else if constexpr (k == 16) { _Pragma("unroll") for (int i = 0; i < 4; ++i) { ho[i] = san(ho[i]); PIN(ho[i]); } }
        else stg(widen(ho[0], ho[1], ho[2], ho[3]), rs, vh + t * 512, shb);
      };
      unsigned m0 = 0, m1 = 0;
      const v4u rs4 = mkrs(wsp, WS_BYTES);
#define WDN(g) asm volatile("s_cmp_lg_u32 %12, 0\n\ts_cbranch_scc1 .Lwd%=\n\t"                                                            \
      "buffer_load_dwordx4 %0, %8, %9, %10 offen sc1\n\tbuffer_load_dwordx4 %1, %8, %9, %11 offen sc1\n\t"                                 \
      "buffer_load_dwordx4 %2, %8, %9, %10 offen offset:2048 sc1\n\tbuffer_load_dwordx4 %3, %8, %9, %11 offen offset:2048 sc1\n\t"         \
      "buffer_load_dwordx4 %4, %13, %9, %10 offen sc1\n\tbuffer_load_dwordx4 %5, %13, %9, %11 offen sc1\n\t"                               \
      "buffer_load_dwordx4 %6, %13, %9, %10 offen offset:2048 sc1\n\tbuffer_load_dwordx4 %7, %13, %9, %11 offen offset:2048 sc1\n.Lwd%=:"  \
      : "+a"(wdn[4 * (g)][0]), "+a"(wdn[4 * (g)][1]), "+a"(wdn[4 * (g) + 1][0]), "+a"(wdn[4 * (g) + 1][1]),                                 \
        "+a"(wdn[4 * (g) + 2][0]), "+a"(wdn[4 * (g) + 2][1]), "+a"(wdn[4 * (g) + 3][0]), "+a"(wdn[4 * (g) + 3][1])                          \
      : "v"(lane16), "s"(rs4), "s"(sdn + (unsigned)(w & 1) * 1024 + (g) * 8192), "s"(sdn + (unsigned)((w & 1) ^ 1) * 1024 + (g) * 8192), "s"(l), "v"(lane16 + 4096) : "memory", "scc")
      v4u pf;
      sfor<0, 140>([&](auto N) {
        constexpr int n = decltype(N)::value, t = 2 + n / 28, s = n % 28;
        if constexpr (n % 20 == 0) STAMPD(53 + n / 20);
        if constexpr (s == 0) ac[t] = mfma(cfd[t >> 2], corr(t), f32x16{});
        if constexpr (n == 21 || n == 47 || n == 76) pf = peek();
        if constexpr (n == 23) need(pf, P_G2 + 16 * l);
        if constexpr (n == 40) { vmwait<10>(); prog(P_T4 + 16 * l); }
        if constexpr (n == 49) need(pf, P_T4 + 16 * l);
        if constexpr (n == 70) { vmwait<1>(); prog(P_T56 + 16 * l); }
        if constexpr (n == 78) need(pf, P_T56 + 16 * l);
        ac[t] = mfma(wb[n % 5], xf[s], ac[t]);
        if constexpr (n + 5 < 140) wb[n % 5] = LDSF(FKB(n + 5));
        constexpr int et = n < 2 ? -1 : n < 20 ? 0 : n < 38 ? 1 : n < 56 ? 2 : n < 58 ? -1 : n < 76 ? 3 : n < 86 ? -1 : n < 104 ? 4 : n < 114 ? -1 : n < 132 ? 5 : -1;
        constexpr int e0 = et == 0 ? 2 : et == 1 ? 20 : et == 2 ? 38 : et == 3 ? 58 : et == 4 ? 86 : 114;
        if constexpr (et >= 0) epi_item(IC<et>{}, IC<n - e0>{});
        if constexpr (n >= 24 && n < 52 && (n & 3) == 0) dmak<2>(g56 + (n - 24) * 512, (unsigned)(w * 14336 + (n - 24) * 512), lane16);
        if constexpr (n == 52) { if (l == 0) { poison_of(1, 0); poison_of(1, 1); dmak<4>(gq + (56 + 7 * w) * 1024, (unsigned)(56 + 7 * w) * 1024, lane16); dmak<3>(gq + (60 + 7 * w) * 1024, (unsigned)(60 + 7 * w) * 1024, lane16); } }
        if constexpr (n == 1) WDN(0);
        if constexpr (n == 79) WDN(1);
        if constexpr (n == 85) WDN(2);
        if constexpr (n == 105) WDN(3);
        if constexpr (n == 113) WDN(4);
        if constexpr (n >= HST - 16 && n < HST && (n & 1) == 0) HLOAD(0, (n - HST + 16) / 2);
        if constexpr (n >= HST + 7 && n < HST + 15) { const v4u v = hf[0][n - HST - 7]; m0 = umax(umax(m0, v.x), v.y); m1 = umax(umax(m1, v.z), v.w); }
        if constexpr (n == HST + 15) {
          if (__builtin_expect(__any(umax(m0, m1) == POISON), 0)) {
#define REF_(i) hf[0][i]
#define REL_(i) HLOAD(0, i)
            VALIDATES(8, REF_, REL_, 50);
#undef REF_
#undef REL_
          }
          m0 = 0; m1 = 0;
        }
        if constexpr (n >= HST + 16 && (n & 1) == 0 && (n - HST) / 2 < 32) { constexpr int hs = (n - HST) / 2; HLOAD(hs >> 3, hs & 7); }
        sb();
      });
#undef WDN
      prog(P_DD + 16 * l);
      STAMP(12 + 16 * l);
      FRESH();
#define VSITE 50
      f32x16 acc[4];
      v4u wd5[8][2];
#define WDLD(i) wd5[(i) >> 1][(i) & 1] = ldg(rs, lane16 + (((i) >> 1) & 1) * 2048, sdn + (unsigned)(((i) & 1) ^ (w & 1)) * 1024 + ((i) >> 2) * 4096 + 40960)
      sfor<0, 7>([&](auto GG) {
        constexpr int g = decltype(GG)::value;
        if (__builtin_expect(__any(umax(m0, m1) == POISON), 0)) {
          asm volatile("s_nop 15\n\ts_nop 7" ::: "memory");
#define REF_(i) hf[g % 4][i]
#define REL_(i) HLOAD(g, i)
          VALIDATE(8, REF_, REL_);
#undef REF_
#undef REL_
        }
        STAMPE(53 + g);
        m0 = 0; m1 = 0;
        if constexpr (g < 6) {
          if constexpr (g == 5) need(pf, P_DD + 16 * l);
          sfor<0, 16>([&](auto II) {
            constexpr int i = decltype(II)::value, a = i & 3, k = i >> 2, e = 16 * g + i;
            acc[a] = mfma(g < 5 ? wdn[(g < 5 ? g : 0) * 4 + k][a & 1] : wd5[k][a & 1], hf[g % 4][2 * k + (a >> 1)], e < 4 ? f32x16{} : acc[a]);
            if constexpr (e >= 2 && e < 20) epi_item(IC<6>{}, IC<e - 2>{});
            if constexpr ((i & 1) == 0) {
              constexpr int hs = (140 - HST) / 2 + e / 2;
              if constexpr (g < 2 && hs < 32) HLOAD(hs >> 3, hs & 7);
              if constexpr (g >= 2 && g < 5) HLOAD(g + 2, i >> 1);
            } else {
              if constexpr (g == 1) WDLD(i >> 1);
              if constexpr (e >= 72 && e < 88) WDLD(8 + (e - 72) / 2);
              if constexpr (e == 77) pf = peek();
              if constexpr (e == 89 || e == 95) { if (l == 0) { constexpr int f = e == 89 ? 0 : 7; dmak<4>(gq + (14 * w + f) * 1024, (unsigned)(14 * w + f) * 1024, lane16); dmak<3>(gq + (14 * w + f + 4) * 1024, (unsigned)(14 * w + f + 4) * 1024, lane16); } }
            }
            if constexpr (i >= 8) { const v4u v = hf[(g + 1) % 4][i - 8]; m0 = umax(umax(m0, v.x), v.y); m1 = umax(umax(m1, v.z), v.w); }
            sb();
          });
        } else {
#pragma unroll
          for (int aa = 1; aa < 5; ++aa) {
            const int a = aa & 3;
#pragma unroll
            for (int k = 0; k < 4; ++k) acc[a] = mfma(wd5[4 + k][a & 1], hf[g % 4][2 * k + (a >> 1)], acc[a]);
            if (aa > 1) red_put(acc[aa - 1], aa - 1, lane16);
            sb();
          }
        }
      });
#undef VSITE
#undef HLOAD
#undef WDLD
      STAMP(13 + 16 * l);
      FRESH();
      finish(acc[0], 2, l == 1);
    }
    STAMP(14 + 16 * l);
    if (l) {
#pragma unroll
      for (int i = 0; i < 28; ++i) asm volatile("" : "=v"(xf[i]));
      asm volatile("" : "=a"(ak), "=a"(av), "=v"(sa.m), "=v"(sa.s), "=v"(sa.rs));
#pragma unroll
      for (int ks = 0; ks < 8; ++ks) asm volatile("" : "=v"(wf[ks][0]), "=v"(wf[ks][1]));
      break;
    }
    {
      vmwait<1>();
      STAMP(19);
      FRESH();
      const unsigned vx_a = lxpat(opq(lane), 56 * 256), vst = lstpat(opq(lane));
      const unsigned so = S_XA(2), so2 = S_STA(2);
      unsigned tb[4], vx[4];
      { const unsigned t0 = trbase();
#pragma unroll
        for (int j = 0; j < 4; ++j) { tb[j] = t0 + (unsigned)(j ^ w) * 2048; vx[j] = vx_a + (unsigned)(j ^ w) * 512; } }
      ak = f32x16{}; av = f32x16{};
#define XLD(s) xf[s] = ldg(rs, vx[(s) & 3] + ((s) & 4) * 512, so + ((s) >> 3) * 4096)
#define RD_(i, m) trf(L + tb[(i) & 3] + ((i) >> 2) * 8192 + (m) * 1024, 256)
#define M0_(wv, xv) ak = mfma(wv, xv, ak)
#define M1_(wv, xv) av = mfma(xv, wv, av)
#define VSITE 45
#define PRE_ rawbar()
      const unsigned swo = O_WOT + (unsigned)(opqs(cg) * 4 + w) * 16384;
#define HOOK_(s) do { if (s >= 12) wf[(s - 12) >> 1][s & 1] = ldg(rs, lane16 + (((s - 12) >> 1) & 1) * 2048, swo + (unsigned)((s & 1) ^ (w & 1)) * 1024 + ((s - 12) >> 2) * 4096); } while (0)
      GEMM2(RD_, M0_, M1_, sa);
#undef VSITE
#undef PRE_
#undef HOOK_
#undef RD_
#undef M0_
#undef M1_
#undef XLD
    }
  }
  if (b == 0 && tid == 0) *(GAS u64*)(p.ws + O_CTRL) = ((((u64)thi << 32) | tlo) >> 8 ^ 0x1E3779B97F4A7C15ull) + 1ull;
  asm volatile(".Lmega_code_end:");
  STAMP(61); STAMPC(63);
}

}

extern "C" void kernel_launch(void* const* d_in, const int* in_sizes, int n_in, void* d_out, int out_size, void* d_ws,
                              size_t ws_size, hipStream_t stream) {
  Params p;
  p.x = (const unsigned short*)d_in[0];
  p.wqkv = (const unsigned short*)d_in[1];
  p.wphi = (const unsigned short*)d_in[2];
  p.wo = (const unsigned short*)d_in[3];
  p.wg = (const unsigned short*)d_in[5];
  p.wu = (const unsigned short*)d_in[6];
  p.wd = (const unsigned short*)d_in[7];
  p.out = (unsigned short*)d_out;
  p.ws = (unsigned char*)d_ws;
  (void)hipFuncSetAttribute((const void*)mega, hipFuncAttributeMaxDynamicSharedMemorySize, LDS_BYTES);
  hipLaunchKernelGGL(mega, dim3(256), dim3(256), LDS_BYTES, stream, p);
}
```

```cpp
#include <hip/hip_runtime.h>
#include <stdint.h>

namespace {

typedef unsigned v4u __attribute__((ext_vector_type(4)));
typedef unsigned v2u __attribute__((ext_vector_type(2)));
typedef float f32x4 __attribute__((ext_vector_type(4)));
typedef float f32x2 __attribute__((ext_vector_type(2)));
typedef float f32x16 __attribute__((ext_vector_type(16)));
typedef short bf16x8 __attribute__((ext_vector_type(8)));
typedef short s16x4 __attribute__((ext_vector_type(4)));
typedef __bf16 bf16x2 __attribute__((ext_vector_type(2)));
typedef unsigned long long u64;
#define LAS __attribute__((address_space(3)))
#define GAS __attribute__((address_space(1)))
#define DI __device__ __forceinline__
typedef __amdgpu_buffer_rsrc_t rsrc_t;

#define STAMPS 0
#define SLA 3
#define SLD 3

constexpr unsigned O_CTRL = 0;
constexpr unsigned O_QKVI = 32768;
constexpr unsigned O_WOT = O_QKVI + 16 * 84 * 1024;
constexpr unsigned O_GUT = O_WOT + 448 * 1024;
constexpr unsigned O_DNT = O_GUT + 3136 * 1024;
constexpr unsigned O_CSG = O_DNT + 1568 * 1024;
constexpr unsigned XSZ = 128 * 56 * 256;
constexpr unsigned O_X = O_CSG + 131072;
constexpr unsigned STSZ = 64 * 14 * 256;
constexpr unsigned O_ST = O_X + 4 * XSZ;
constexpr unsigned KVPSZ = 16 * 16 * 8192;
constexpr unsigned O_KVP = O_ST + 4 * STSZ;
constexpr unsigned KVRSZ = 16 * 4096;
constexpr unsigned O_KVR = O_KVP + 2 * KVPSZ;
constexpr unsigned OFSZ = 128 * 64 * 256;
constexpr unsigned O_OF = O_KVR + 2 * KVRSZ;
constexpr unsigned HBSZ = 128 * 224 * 256;
constexpr unsigned O_HB = O_OF + 2 * OFSZ;
constexpr unsigned O_DUMST = O_HB + 2 * HBSZ;
constexpr unsigned O_STAMP = O_DUMST + STSZ;
constexpr unsigned WS_BYTES = O_STAMP + 256 * 4 * 64 * 8;

constexpr int L_SIG = 111 * 1024;
constexpr int L_SCR = 112 * 1024;
constexpr int L_SCA = 116 * 1024, SCA_ST = 12288;
constexpr int LDS_BYTES = 160 * 1024;
enum { P_J = 10, P_G2 = 18, P_T4 = 20, P_T56 = 21, P_DD = 22 };

constexpr unsigned POISON = 0xFFFFFFFFu;
constexpr int SPIN_CAP = 1 << 16;

struct Params {
  const unsigned short* x;
  const unsigned short* wqkv;
  const unsigned short* wphi;
  const unsigned short* wo;
  const unsigned short* wg;
  const unsigned short* wu;
  const unsigned short* wd;
  unsigned short* out;
  unsigned char* ws;
};

DI v4u ldg(rsrc_t rs, unsigned voff, unsigned soff) { return __builtin_amdgcn_raw_buffer_load_b128(rs, voff, soff, 16); }
DI v2u ldg2(rsrc_t rs, unsigned voff, unsigned soff) { return __builtin_amdgcn_raw_buffer_load_b64(rs, voff, soff, 16); }
DI void stg(v4u v, rsrc_t rs, unsigned voff, unsigned soff) { __builtin_amdgcn_raw_buffer_store_b128(v, rs, voff, soff, 16); }
DI void stg2(v2u v, rsrc_t rs, unsigned voff, unsigned soff) { __builtin_amdgcn_raw_buffer_store_b64(v, rs, voff, soff, 16); }
DI unsigned san(unsigned x) { return x < 0xFFFFFFFEu ? x : 0xFFFFFFFEu; }
DI unsigned pk(float a, float b) { f32x2 v = {a, b}; return __builtin_bit_cast(unsigned, __builtin_convertvector(v, bf16x2)); }
DI float blo(unsigned u) { return __builtin_bit_cast(float, u << 16); }
DI float bhi(unsigned u) { return __builtin_bit_cast(float, u & 0xFFFF0000u); }
DI float bfr(float a) { return blo(pk(a, a)); }
DI float u2f(unsigned u) { return __builtin_bit_cast(float, u); }
DI unsigned f2u(float f) { return __builtin_bit_cast(unsigned, f); }
DI f32x16 mfma(v4u a, v4u b, f32x16 c) {
  return __builtin_amdgcn_mfma_f32_32x32x16_bf16(__builtin_bit_cast(bf16x8, a), __builtin_bit_cast(bf16x8, b), c, 0, 0, 0);
}
DI void mfv(f32x16& c, v4u a, v4u b) { asm volatile("v_mfma_f32_32x32x16_bf16 %0, %1, %2, %0" : "+v"(c) : "v"(a), "v"(b)); }
DI void mfvn(f32x16& c, v4u a, v4u b) { asm volatile("s_nop 1\n\tv_mfma_f32_32x32x16_bf16 %0, %1, %2, %0" : "+v"(c) : "v"(a), "v"(b)); }
DI void mfv0(f32x16& c, v4u a, v4u b) { asm volatile("s_nop 1\n\tv_mfma_f32_32x32x16_bf16 %0, %1, %2, 0" : "=&v"(c) : "v"(a), "v"(b)); }
DI unsigned umax(unsigned a, unsigned b) { return a > b ? a : b; }
DI unsigned mx4(unsigned m, v4u v) { asm("v_max3_u32 %0, %0, %1, %2\n\tv_max3_u32 %0, %0, %3, %4" : "+v"(m) : "v"(v.x), "v"(v.y), "v"(v.z), "v"(v.w)); return m; }
DI void bar() { asm volatile("s_waitcnt lgkmcnt(0)" ::: "memory"); __builtin_amdgcn_s_barrier(); asm volatile("" ::: "memory"); }
DI void drain() { asm volatile("s_waitcnt vmcnt(0)" ::: "memory"); }
DI void sb() { __builtin_amdgcn_sched_barrier(0); }
DI unsigned opq(unsigned v) { asm volatile("" : "+v"(v)); return v; }
DI float sel(bool c, float a, float b) { asm volatile("" : "+v"(a), "+v"(b)); return c ? a : b; }
DI int opqs(int v) { asm volatile("" : "+s"(v)); return v; }
DI float max3(float a, float b, float c) { float d; asm("v_max3_f32 %0, %1, %2, %3" : "=v"(d) : "v"(a), "v"(b), "v"(c)); return d; }
DI float min3(float a, float b, float c) { float d; asm("v_min3_f32 %0, %1, %2, %3" : "=v"(d) : "v"(a), "v"(b), "v"(c)); return d; }
DI float rcp(float x) { return __builtin_amdgcn_rcpf(x); }
DI float hsum(float x) { auto r = __builtin_amdgcn_permlane32_swap(f2u(x), f2u(x), false, false); return u2f(r[0]) + u2f(r[1]); }
DI v4u widen(unsigned ax, unsigned ay, unsigned bx, unsigned by) {
  auto r0 = __builtin_amdgcn_permlane32_swap(ax, bx, false, false);
  auto r1 = __builtin_amdgcn_permlane32_swap(ay, by, false, false);
  return v4u{r0[0], r1[0], r0[1], r1[1]};
}
template <int N> DI void dmak(v4u rs, unsigned so, unsigned l, unsigned voff, int k = 0) {
#define DMA_(j) "buffer_load_dwordx4 %1, %2, %3 offen offset:%" #j " sc1 lds\n\t"
  if constexpr (N == 1) asm volatile("s_nop 0\n\t" DMA_(4) :: "{m0}"(l), "v"(voff), "s"(rs), "s"(so), "i"(k * 1024) : "memory");
  else if constexpr (N == 2) asm volatile("s_nop 0\n\t" DMA_(4) DMA_(5) :: "{m0}"(l), "v"(voff), "s"(rs), "s"(so), "i"(k * 1024), "i"(k * 1024 + 1024) : "memory");
  else if constexpr (N == 3) asm volatile("s_nop 0\n\t" DMA_(4) DMA_(5) DMA_(6) :: "{m0}"(l), "v"(voff), "s"(rs), "s"(so), "i"(k * 1024), "i"(k * 1024 + 1024), "i"(k * 1024 + 2048) : "memory");
  else asm volatile("s_nop 0\n\t" DMA_(4) DMA_(5) DMA_(6) DMA_(7) :: "{m0}"(l), "v"(voff), "s"(rs), "s"(so), "i"(0), "i"(1024), "i"(2048), "i"(3072) : "memory");
#undef DMA_
}
DI void dmab(v4u rsrc, unsigned voff, unsigned soff, unsigned l) {
  asm volatile("s_nop 0\n\tbuffer_load_dwordx4 %1, %2, %3 offen lds" :: "{m0}"(l), "v"(voff), "s"(rsrc), "s"(soff) : "memory");
}
DI void dmabn(v4u rsrc, unsigned voff, unsigned soff, unsigned l) {
  asm volatile("s_nop 0\n\tbuffer_load_dwordx4 %1, %2, %3 offen nt lds" :: "{m0}"(l), "v"(voff), "s"(rsrc), "s"(soff) : "memory");
}
DI v4u mkrs(const void* p, unsigned bytes) {
  return v4u{(unsigned)__builtin_amdgcn_readfirstlane((unsigned)(u64)p), (unsigned)__builtin_amdgcn_readfirstlane((unsigned)((u64)p >> 32)) & 0xFFFFu,
             (unsigned)__builtin_amdgcn_readfirstlane(bytes), 0x00020000u};
}
DI v4u trf(const LAS unsigned char* a, int d) {
  const s16x4 lo = __builtin_amdgcn_ds_read_tr16_b64_v4i16((LAS s16x4*)a);
  const s16x4 hh = __builtin_amdgcn_ds_read_tr16_b64_v4i16((LAS s16x4*)(a + d));
  return __builtin_bit_cast(v4u, __builtin_shufflevector(lo, hh, 0, 1, 2, 3, 4, 5, 6, 7));
}
template <int N> DI void vmwait() { asm volatile("s_waitcnt vmcnt(%0)" :: "n"(N) : "memory"); }
DI void rawbar() { asm volatile("" ::: "memory"); __builtin_amdgcn_s_barrier(); asm volatile("" ::: "memory"); }

#define RETRY(i) do { } while (0)
#define STAMP(i) do { } while (0)
#define STAMPC(i) do { } while (0)

#define STAMPD(i) do { } while (0)
#define STAMPP(i) do { } while (0)
#define STAMPG(i) do { } while (0)
#define STAMPE(i) STAMP(i)
#define REFETCH(N, REF, RELOAD, SITE)                                         \
  _Pragma("unroll 1") for (int sp_ = 0;; ++sp_) {                             \
    RETRY(SITE);                                                              \
    asm volatile("" ::: "memory");                                            \
    _Pragma("unroll") for (int i_ = 0; i_ < (N); ++i_) RELOAD(i_);            \
    unsigned n_ = 0;                                                          \
    _Pragma("unroll") for (int i_ = 0; i_ < (N); ++i_) n_ = mx4(n_, REF(i_)); \
    if (!__any(n_ == POISON)) break;                                          \
    if (dead || sp_ > SPIN_CAP) { dead = 1; break; }                          \
    __builtin_amdgcn_s_sleep(1);                                              \
  }
#define VALIDATES(N, REF, RELOAD, SITE)                                       \
  do {                                                                        \
    unsigned m_ = 0;                                                          \
    _Pragma("unroll") for (int i_ = 0; i_ < (N); ++i_) m_ = mx4(m_, REF(i_)); \
    if (__builtin_expect(__any(m_ == POISON), 0)) { REFETCH(N, REF, RELOAD, SITE); } \
  } while (0)

template <int OFF> DI void lda(v4u& d, v4u rs, unsigned vo, unsigned so) { asm volatile("buffer_load_dwordx4 %0, %1, %2, %3 offen offset:%4 sc1" : "=a"(d) : "v"(vo), "s"(rs), "s"(so), "n"(OFF) : "memory"); }
template <int OFF> DI void ldv(v4u& d, v4u rs, unsigned vo, unsigned so) { asm volatile("buffer_load_dwordx4 %0, %1, %2, %3 offen offset:%4 sc1" : "=v"(d) : "v"(vo), "s"(rs), "s"(so), "n"(OFF) : "memory"); }
#define MFX(NAME, CA, CB) \
  template <bool Z> DI void NAME(f32x16& c, v4u a, v4u b) { \
    if constexpr (Z) asm volatile("v_mfma_f32_32x32x16_bf16 %0, %1, %2, 0" : "=&v"(c) : CA(a), CB(b)); \
    else asm volatile("v_mfma_f32_32x32x16_bf16 %0, %1, %2, %0" : "+v"(c) : CA(a), CB(b)); }
MFX(mf_aa, "a", "a") MFX(mf_av, "a", "v") MFX(mf_va, "v", "a") MFX(mf_vv, "v", "v")
enum { K_NONE, K_HS, K_HL, K_W5, K_D56, K_DQ, K_DK, K_PH, K_PX, K_PO };
constexpr int sched(int l, int u) {
  constexpr int hs[7] = {19, 37, 55, 75, 103, 131, 159}, hl[7] = {76, 92, 114, 130, 158, 178, 196};
  for (int t = 0; t < 7; ++t) {
    if (u == hs[t]) return K_HS * 64 + t;
    if (u >= hl[t] && u < hl[t] + 16 && !(u & 1)) return K_HL * 64 + t * 8 + (u - hl[t]) / 2;
  }
  if (u >= 24 && u < 52 && (u & 3) == 0) return K_D56 * 64 + (u - 24) / 4;
  if (u >= 54 && u < 72 && (u - 54) % 3 == 0) return K_W5 * 64 + (u - 54) / 3;
  if (u == 151 || u == 194) return K_W5 * 64 + (u == 151 ? 6 : 7);
  if (u >= 146 && u < 158 && !(u & 1)) return K_W5 * 64 + 8 + (u - 146) / 2;
  if (u == 174 || u == 176) return K_W5 * 64 + 14 + (u - 174) / 2;
  if (l == 0) {
    if (u == 4 || u == 10 || u == 16) return K_PX * 64 + (u - 4) / 6;
    if (u == 22 || u == 30 || u == 38) return K_PH * 64 + 4 + (u - 22) / 8;
    if (u == 42 || u == 50) return K_PO * 64 + (u - 42) / 8;
    if (u >= 85 && u < 141 && (u - 85) % 8 == 0) return K_DQ * 64 + (u - 85) / 8;
    if (u >= 220 && u < 248 && !(u & 1)) return K_DK * 64 + (u - 220) / 2;
  }
  return 0;
}
constexpr int cum(int l, int u) { int n = 0; for (int k = 0; k < u; ++k) { const int c = sched(l, k) / 64; n += c == K_NONE ? 0 : c == K_D56 ? 2 : 1; } return n; }
constexpr int younger(int l, int p, int c) { return cum(l, c) - cum(l, p + 1); }
static_assert(cum(0, 252) == 122 && cum(1, 252) == 93, "schedule entries collide");
constexpr int NJ = 5, PB = 19;
constexpr int sat(int s) { return (!(s & 1) && NJ + s / 2 < 12) ? NJ + s / 2 : -1; }
constexpr int qcount(int e) { int n = 0; for (int j = 0; j < 7; ++j) n += 13 + 2 * j < e; return n; }
static_assert(qcount(28) == 7, "q weights");
constexpr int xpos(int i) { constexpr int a[7] = {-4, -4, -3, 0, 2, 4, 13}; return a[i]; }
constexpr int wpos(int i) { constexpr int a[7] = {-4, -3, -3, -2, -2, -1, -1}; return a[i]; }
constexpr int xrd(int i) { constexpr int a[7] = {-1, -1, 0, 10, 14, 18, 22}; return a[i]; }
enum { E_JW, E_PL, E_STEP, E_XEND, E_WEND, E_PEND };
constexpr int walk(int ev, int arg) {
  int n = 0;
  for (int p = -4; p < 0; ++p) {
    if (p == -3 && ev == E_JW) return n;
    if (p == -1) n += NJ + 2;
    for (int i = 0; i < 7; ++i) {
      if (xpos(i) == p) { n += 4; if (ev == E_XEND && arg == i) return n; }
      if (wpos(i) == p) { n += 2; if (ev == E_WEND && arg == i) return n; }
    }
  }
  if (ev == E_PL) return n;
  for (int s = 0; s < 28; ++s) {
    if (ev == E_STEP && arg == s) return n;
    if (s == PB) ++n;
    if (sat(s) >= 0) { ++n; if (ev == E_PEND && sat(s) == 11) return n; }
    if (qcount(s + 1) > qcount(s)) ++n;
    for (int i = 0; i < 7; ++i) {
      if (xpos(i) >= 0 && (s == xpos(i) || s == xpos(i) + 1)) { n += 2; if (ev == E_XEND && arg == i && s == xpos(i) + 1) return n; }
      if (wpos(i) == s) { n += 2; if (ev == E_WEND && arg == i) return n; }
    }
  }
  return n;
}
constexpr int mx2(int a, int b) { return a > b ? a : b; }
constexpr int wlast(int a, int b) { int m = 0; for (int i = a; i < b; ++i) m = mx2(m, walk(E_WEND, i)); return m; }
constexpr bool cp_ok() {
  for (int i = 0; i < 7; ++i) {
    if (i >= 3 && xpos(i) < xrd(i - 3) + (xrd(i - 3) < 0 ? 1 : 2)) return false;
    if (xrd(i) >= 0 && (xrd(i) + 1 >= 4 * i || xrd(i) <= xpos(i) + 1)) return false;
    if (xrd(i) < 0 && xpos(i) >= 0) return false;
    if (wpos(i) >= (i < 2 ? 0 : i < 4 ? 4 : 12)) return false;
  }
  return true;
}
static_assert(cp_ok(), "chunk schedule");
template <int V> struct IC { static constexpr int value = V; constexpr operator int() const { return V; } };
template <int B, int E, class F> DI void sfor(F&& f) { if constexpr (B < E) { f(IC<B>{}); sfor<B + 1, E>(f); } }
struct Stats { float m, s, rs; };
struct LdsBase { DI LAS unsigned char* operator+(unsigned a) const { return (LAS unsigned char*)(size_t)a; } };
DI unsigned lxpat(unsigned ln, unsigned rowb) { return ((ln >> 4) & 1) * rowb + (ln & 15) * 16 + (ln >> 5) * 256; }
DI unsigned lstpat(unsigned ln) { return (ln & 31) * 8 + (ln >> 5) * 256; }

__global__ __launch_bounds__(256, 1) void mega(Params p) {
  const LdsBase L;
  const int tid = threadIdx.x, lane = tid & 63;
  const int b = blockIdx.x, idx = b >> 3;
  int w = __builtin_amdgcn_readfirstlane(tid >> 6), r = ((b & 7) >> 1) * 4 + (idx >> 3), c = (b & 1) * 8 + (idx & 7);
  const int hi = lane >> 5;
  const rsrc_t rs = __builtin_amdgcn_make_buffer_rsrc(p.ws, 0, WS_BYTES, 0x00020000);
  int dead = 0;
  u64 code_pc; unsigned code_len;
  asm volatile("s_getpc_b64 %0\n.Lmega_code_beg:\n\ts_mov_b32 %1, .Lmega_code_end-.Lmega_code_beg" : "=s"(code_pc), "=s"(code_len));
  if ((int)code_len < 0) code_len = 0;
  STAMP(0); STAMPC(62);

  const bool act = (c & 7) != 7;
  const int th = c >> 3;
  int cg = act ? (c & 7) : 0;
  int q = (b & 7) * 32 + idx;
  int T32a = 4 * r + w;
  int T32o = 4 * r + 2 * th + (w >> 1);
  const v4u rs4 = mkrs(p.ws, WS_BYTES);
#define JIT_() do { } while (0)
#define FRESH() JIT_(); asm volatile("" : "+s"(w), "+s"(c), "+s"(r), "+s"(cg), "+s"(q), "+s"(T32a), "+s"(T32o))
#define S_XA(xb) (O_X + (xb) * XSZ + (unsigned)opqs(T32a) * (2 * 56 * 256))
#define S_STA(xb) (O_ST + (xb) * STSZ + (unsigned)opqs(T32a) * (14 * 256))
#define S_OFST(l) (O_OF + (l) * OFSZ + (unsigned)opqs(T32a) * (2 * 64 * 256) + (unsigned)opqs(c) * (4 * 256))
#define S_HBST(l) (O_HB + (l) * HBSZ + (unsigned)opqs(T32a) * (2 * 224 * 256) + (unsigned)opqs(c) * (14 * 256))
#define S_XST ((unsigned)opqs(T32o) * (2 * 56 * 256) + (unsigned)(8 * opqs(cg) + 4 * (w & 1)) * 256)
#define S_STST ((unsigned)(opqs(T32o) * 14 + 2 * opqs(cg) + (w & 1)) * 256)
#define S_KVP(l) (O_KVP + (l) * KVPSZ + (unsigned)(opqs(c) * 16 + opqs(r)) * 8192)
#define S_KVRST(l) (O_KVR + (l) * KVRSZ + (unsigned)(opqs(c) * 4096 + opqs(r) * 256))
  constexpr unsigned OOB = 0x80000000u;
#define WDIS (!act ? 0x40000000u : 0u)
  constexpr unsigned ONE2 = 0x3F803F80u;
#define P4 ([]() { v4u v_; asm volatile("v_mov_b32 %0, -1\n\tv_mov_b32 %1, -1\n\tv_mov_b32 %2, -1\n\tv_mov_b32 %3, -1" : "=v"(v_.x), "=v"(v_.y), "=v"(v_.z), "=v"(v_.w)); return v_; }())

  JIT_();
  const v4u rqkv = mkrs(p.wqkv, 448 * 1344 * 2), rx = mkrs(p.x, 2048 * 448 * 2);
  const unsigned vwq = (unsigned)(lane >> 2) * 2688 + ((lane & 3) == 3 ? 40u : (unsigned)(lane & 3) * 16) + (unsigned)c * 56;
  const unsigned vx0 = (unsigned)(lane >> 3) * 896 + (((unsigned)lane & 7) ^ (unsigned)(lane >> 3)) * 16;
  const unsigned jsb = (unsigned)(84 * 1024 + 7168 * w);
  const unsigned xsb = (unsigned)(112 + 12 * w) * 1024;
  v4u xf[28];
  unsigned warm;
  {
    const unsigned u = (unsigned)(idx * 4 + w), pr = (unsigned)(b >> 1) & 3;
    const unsigned t = min(u * 11 + ((unsigned)lane - 28 & 15), 1343u), pp = t >> 2, co = u * 1024 + (lane & 7) * 128;
    const unsigned ox = opq(pr * (512 * 896) + (u * 28 + lane) * 128);
    const unsigned ow = opq((pp + 9 * ((pp * 171) >> 9) + 3 * pr) * 896 + (unsigned)(b & 1) * 384 + (t & 3) * 128);
    const unsigned oc = opq(co + 16 <= code_len ? co : 0u), op = (unsigned)(b & 1) * 12544 + min(u, 97u) * 128;
    u64 ba = lane < 28 ? (u64)p.x : (u64)p.wqkv, bb = lane < 47 ? code_pc : (u64)p.wphi;
    asm volatile("" : "+v"(ba), "+v"(bb));
    const u64 pa = (lane < 39 ? ba : bb) + (lane < 28 ? ox : lane < 39 ? ow : lane < 47 ? oc : op);
    asm volatile("global_load_dword %0, %1, off" : "=v"(warm) : "v"(pa) : "memory");
  }
  unsigned warm2;
  asm volatile("buffer_load_dword %0, %1, %2, %3 offen sc1" : "=v"(warm2) : "v"(lane < 2 ? (unsigned)lane * 128 : OOB), "s"(rs4), "s"(O_X + (unsigned)(b & 7) * 32768 + (unsigned)(idx * 4 + w) * 256) : "memory");
  const int hd = idx & 7, sm = (b & 1) ? 0x2A : 0x54, ns = (idx >> 3) * 5 + __builtin_popcount(~sm & ((1 << hd) - 1));
  const bool str = (sm >> hd) & 1;
  const bool jgu = !str && ns < 14, has_job = !str || w == 1;
  const int jq = (b & 7) * 14 + ns;
  const int jj = (b & 7) * 36 + (str ? 24 + idx - ns : (ns - 14) * 4 + w);
  {
    v4u rj0, rj1; unsigned jso, jsd = 224, jld = 896;
    if (jgu) { const int cc = jq / 7, s = 4 * (jq - 7 * cc) + w; rj0 = mkrs(p.wg, 448 * 1792 * 2); rj1 = mkrs(p.wu, 448 * 1792 * 2); jld = 3584; jso = s * 16 * 3584 + cc * 224; jsd = 0; }
    else if (jj < 224) { rj0 = rj1 = mkrs(p.wd, 1792 * 448 * 2); jso = (jj >> 1) * 16 * 896 + (jj & 1) * 448; }
    else { const int hp = (jj - 224) >> 1; rj0 = rj1 = mkrs(p.wo, 448 * 448 * 2); jso = (28 * (hp >> 1) + 16 * (hp & 1)) * 896 + (jj & 1) * 448; }
#pragma unroll
    for (int k = 0; k < 4; ++k) {
      const unsigned pc = k * 64 + lane, row = pc / 14, vo = has_job ? row * jld + (pc - row * 14) * 16 : OOB;
      if (k < 3 || lane < 32) { dmabn(rj0, vo, jso, jsb + k * 1024); dmabn(rj1, vo, jso + jsd, jsb + 3584 + k * 1024); }
    }
  }
  STAMP(40);
  auto xchunk = [&](int lc, int pt) {
    const unsigned sx = (unsigned)T32a * (32 * 896) + lc * 128;
#pragma unroll
    for (int rg = 2 * pt; rg < 2 * pt + 2; ++rg) dmab(rx, rg < 2 ? vx0 : vx0 ^ 16, sx + rg * 7168, xsb + (lc % 3) * 4096 + rg * 1024);
  };
  auto wchunk = [&](int lc) { const unsigned s = 4 * lc + w; dmab(rqkv, vwq, s * 43008 + 896, s * 2048); dmab(rqkv, vwq, s * 43008 + 1792, s * 2048 + 1024); };
  auto chunks = [&](auto P) { sfor<0, 7>([&](auto LC) {
    constexpr int lc = decltype(LC)::value;
    if constexpr (xpos(lc) == decltype(P)::value) { xchunk(lc, 0); xchunk(lc, 1); }
    if constexpr (wpos(lc) == decltype(P)::value) wchunk(lc); }); sb(); };
  unsigned tlo, thi;
  {
    const u64 ep = *(const __attribute__((address_space(4))) u64*)(p.ws + O_CTRL);
    const u64 tagb = (ep ^ 0x9E3779B97F4A7C15ull) << 8;
    tlo = (unsigned)tagb; thi = (unsigned)(tagb >> 32);
  }

  auto prog = [&](unsigned v) { asm volatile("" ::: "memory"); *(volatile LAS unsigned*)(L + L_SIG + w * 4) = v; asm volatile("" ::: "memory"); };
  auto peek = [&]() -> v2u {
    asm volatile("" ::: "memory");
    const unsigned a = L_SIG + (opq(lane) & 3) * 4;
    return v2u{*(volatile LAS unsigned*)(L + a), a};
  };
  auto need = [&](v2u f, unsigned t) {
    unsigned v = f.x, cnt;
    asm volatile("v_cmp_gt_u32 vcc, %3, %0\n\ts_cbranch_vccnz .Lnd%=\n.Lnr%=:\n"
                 ".subsection 1\n.Lnd%=:\n\ts_mov_b32 %1, 0x400000\n.Lnl%=:\n\tds_read_b32 %0, %2\n\ts_waitcnt lgkmcnt(0)\n\t"
                 "v_cmp_gt_u32 vcc, %3, %0\n\ts_cbranch_vccz .Lnr%=\n\t"
                 "s_sub_u32 %1, %1, 1\n\ts_cmp_lg_u32 %1, 0\n\ts_cbranch_scc1 .Lnl%=\n\ts_branch .Lnr%=\n.subsection 0\n"
                 : "+v"(v), "=&s"(cnt) : "v"(f.y), "s"(t) : "memory", "scc", "vcc");
  };
  auto await = [&](unsigned t) { need(peek(), t); };
  auto touch = [&]() -> unsigned {
    const unsigned co = (unsigned)((opqs(b) >> 3) * 4 + w) * 1024 + (opq(lane) & 7) * 128; unsigned v;
    asm volatile("global_load_dword %0, %1, %2" : "=v"(v) : "v"(co + 16 <= code_len ? co : 0u), "s"(code_pc) : "memory");
    return v;
  };
  auto split2 = [&](float x, float y) -> v4u {
    const float x0 = bfr(x), x1 = bfr(x - x0), x2 = (x - x0) - x1;
    const float y0 = bfr(y), y1 = bfr(y - y0), y2 = (y - y0) - y1;
    return v4u{pk(x0, x1), pk(x2, y0), pk(y1, y2), 0u};
  };
  auto mk_stats = [&](float mean, float var) -> Stats {
    Stats o; o.m = bfr(mean);
    o.s = bfr(__builtin_amdgcn_sqrtf(bfr(bfr(var) + 1e-5f)));
    o.rs = rcp(o.s);
    return o;
  };
#define PIN(x) asm volatile("" : "+v"(x))
  float lt0, lt1, lt2;
  auto ln_step = [&](int k, v2u (&st)[7], Stats& o) {
    if (k == 0) { lt0 = ((u2f(st[0].x) + u2f(st[1].x)) + (u2f(st[2].x) + u2f(st[3].x))) + ((u2f(st[4].x) + u2f(st[5].x)) + u2f(st[6].x)); PIN(lt0); }
    if (k == 1) { lt0 = hsum(lt0) * (1.0f / 448.0f); PIN(lt0);
                  lt1 = ((u2f(st[0].y) + u2f(st[1].y)) + (u2f(st[2].y) + u2f(st[3].y))); PIN(lt1); }
    if (k == 2) { lt1 += ((u2f(st[4].y) + u2f(st[5].y)) + u2f(st[6].y)); PIN(lt1);
                  const float d0 = u2f(st[0].x) * (1.0f / 32.0f) - lt0, d1 = u2f(st[1].x) * (1.0f / 32.0f) - lt0; lt2 = d0 * d0; lt2 += d1 * d1; PIN(lt2); }
    if (k == 3) { _Pragma("unroll") for (int i = 2; i < 5; ++i) { const float d = u2f(st[i].x) * (1.0f / 32.0f) - lt0; lt2 += d * d; } PIN(lt2); }
    if (k == 4) { _Pragma("unroll") for (int i = 5; i < 7; ++i) { const float d = u2f(st[i].x) * (1.0f / 32.0f) - lt0; lt2 += d * d; } lt1 += 32.0f * lt2; PIN(lt1); o.m = bfr(lt0); PIN(o.m); }
    if (k == 5) { lt1 = bfr(hsum(lt1) * (1.0f / 448.0f)); PIN(lt1); }
    if (k == 6) { lt1 = __builtin_amdgcn_sqrtf(bfr(lt1 + 1e-5f)); PIN(lt1); }
    if (k == 7) { o.s = bfr(lt1); o.rs = rcp(o.s); PIN(o.s); PIN(o.rs); }
  };

  v4u wdn[20][2];
  v4u wph[2];
  v4u cfa, cfd[2];
  unsigned rsd[8];

  auto red_put = [&](const f32x16& t, int a, unsigned l16) {
    LAS f32x4* d = (LAS f32x4*)(L + L_SCR + ((w ^ a) * 3 + (a - 1)) * 4096 + l16);
#pragma unroll
    for (int qq = 0; qq < 4; ++qq) d[qq * 64] = f32x4{t[4 * qq], t[4 * qq + 1], t[4 * qq + 2], t[4 * qq + 3]};
  };
  auto red_put1 = [&](const f32x16& t, int a, int qq, unsigned l16) {
    ((LAS f32x4*)(L + L_SCR + ((w ^ a) * 3 + (a - 1)) * 4096 + l16))[qq * 64] = f32x4{t[4 * qq], t[4 * qq + 1], t[4 * qq + 2], t[4 * qq + 3]};
  };
  auto finish = [&](f32x16& a0, int xb, bool last, unsigned lrd) {
    bar();
    unsigned o[8];
    f32x4 t[4][3];
#pragma unroll
    for (int qq = 0; qq < 4; ++qq) {
      const LAS f32x4* s = (const LAS f32x4*)(L + lrd) + qq * 64;
      t[qq][0] = s[0]; t[qq][1] = s[256]; t[qq][2] = s[512];
    }
    sb();
    asm volatile("" : "+v"(a0));
    const unsigned ln = opq(lane);
    float res[16];
#pragma unroll
    for (int qq = 0; qq < 8; ++qq) { f32x2 r2 = f32x2{blo(rsd[qq]), bhi(rsd[qq])} + f32x2{a0[2 * qq], a0[2 * qq + 1]}; PIN(r2); res[2 * qq] = r2.x; res[2 * qq + 1] = r2.y; }
    unsigned vst_ = lxpat(ln, 56 * 256);
    unsigned sst = (act ? O_X + xb * XSZ : 0x40000000u) + S_XST;
    const unsigned so2 = act ? O_ST + xb * STSZ : O_DUMST;
    GAS v4u* op = (GAS v4u*)(p.out + (size_t)(32 * T32o + (ln & 31)) * 448 + 64 * cg + 32 * (w & 1) + 8 * (ln >> 5));
    if (last) asm volatile("" : "+v"(op)); else asm volatile("" : "+v"(vst_), "+s"(sst));
    sb();
#pragma unroll
    for (int hf_ = 0; hf_ < 2; ++hf_) {
#pragma unroll
      for (int qq = 2 * hf_; qq < 2 * hf_ + 2; ++qq) {
#pragma unroll
        for (int i = 0; i < 4; ++i) res[4 * qq + i] = (res[4 * qq + i] + t[qq][0][i]) + (t[qq][1][i] + t[qq][2][i]);
        o[2 * qq] = pk(res[4 * qq], res[4 * qq + 1]); o[2 * qq + 1] = pk(res[4 * qq + 2], res[4 * qq + 3]);
        if (!last) { o[2 * qq] = san(o[2 * qq]); o[2 * qq + 1] = san(o[2 * qq + 1]); }
      }
      const v4u pv = widen(o[4 * hf_], o[4 * hf_ + 1], o[4 * hf_ + 2], o[4 * hf_ + 3]);
      if (last) { if (act) op[2 * hf_] = pv; }
      else stg(pv, rs, vst_ + hf_ * 512, sst);
      sb();
    }
    if (last) return;
    sb();
    float s1a = 0.f, s1b = 0.f;
#pragma unroll
    for (int qq = 0; qq < 8; ++qq) { rsd[qq] = o[qq]; res[2 * qq] = blo(o[qq]); res[2 * qq + 1] = bhi(o[qq]); s1a += res[2 * qq]; s1b += res[2 * qq + 1]; }
    const float s1 = hsum(s1a + s1b);
    const float mu = s1 * (1.0f / 32.0f);
    float m2a = 0.f, m2b = 0.f;
#pragma unroll
    for (int i = 0; i < 8; ++i) { const float d = res[2 * i] - mu, e = res[2 * i + 1] - mu; m2a += d * d; m2b += e * e; }
    const float m2 = hsum(m2a + m2b);
    stg2(v2u{san(f2u(s1)), san(f2u(m2))}, rs, ln < 32 ? ln * 8 : OOB, so2 + S_STST);
  };

  auto dual_softmax = [&](const f32x16& a, float sc, float osc, unsigned (&o)[16], auto&& hook) {
    f32x2 b[8];
#pragma unroll
    for (int j = 0; j < 8; ++j) b[j] = f32x2{a[2 * j], a[2 * j + 1]};
#pragma unroll
    for (int j = 6; j < 8; ++j) { PIN(b[j]); b[j] = f32x2{hi ? b[0].x : b[j].x, hi ? b[0].x : b[j].y}; }
    float mx = max3(max3(b[0].x, b[0].y, b[1].x), max3(b[1].y, b[2].x, b[2].y), max3(b[3].x, b[3].y, b[4].x));
    float mn = min3(min3(b[0].x, b[0].y, b[1].x), min3(b[1].y, b[2].x, b[2].y), min3(b[3].x, b[3].y, b[4].x));
    mx = max3(mx, max3(b[4].y, b[5].x, b[5].y), max3(b[6].x, b[6].y, b[7].x));
    mn = min3(mn, min3(b[4].y, b[5].x, b[5].y), min3(b[6].x, b[6].y, b[7].x));
    mx = max3(mx, b[7].y, b[7].y); mn = min3(mn, b[7].y, b[7].y);
    { auto r = __builtin_amdgcn_permlane32_swap(f2u(mx), f2u(mx), false, false); mx = max3(u2f(r[0]), u2f(r[1]), u2f(r[1])); }
    { auto r = __builtin_amdgcn_permlane32_swap(f2u(mn), f2u(mn), false, false); mn = min3(u2f(r[0]), u2f(r[1]), u2f(r[1])); }
    const float k = sc * 1.4426950408889634f;
    const float cx = -mx * k, cn = mn * k, ninf = u2f(0xFF800000u), cxh = hi ? ninf : cx, cnh = hi ? ninf : cn;
    f32x2 ep[8], en[8];
    hook(IC<0>{});
    sfor<0, 4>([&](auto G) {
      constexpr int g = decltype(G)::value;
#pragma unroll
      for (int j = 2 * g; j < 2 * g + 2; ++j) {
        const float c0 = j >= 6 ? cxh : cx, c1 = j >= 6 ? cnh : cn;
        const f32x2 tp = __builtin_elementwise_fma(b[j], f32x2{k, k}, f32x2{c0, c0}), tn = __builtin_elementwise_fma(b[j], f32x2{-k, -k}, f32x2{c1, c1});
        ep[j] = f32x2{__builtin_amdgcn_exp2f(tp.x), __builtin_amdgcn_exp2f(tp.y)};
        en[j] = f32x2{__builtin_amdgcn_exp2f(tn.x), __builtin_amdgcn_exp2f(tn.y)};
        asm volatile("" : "+v"(ep[j]), "+v"(en[j]));
      }
      hook(IC<1 + g>{});
    });
    const f32x2 sp = ((ep[0] + ep[1]) + (ep[2] + ep[3])) + ((ep[4] + ep[5]) + (ep[6] + ep[7]));
    const f32x2 sn = ((en[0] + en[1]) + (en[2] + en[3])) + ((en[4] + en[5]) + (en[6] + en[7]));
    auto r1 = __builtin_amdgcn_permlane32_swap(f2u(sp.x + sp.y), f2u(sn.x + sn.y), false, false);
    const float rr = osc * rcp(u2f(r1[0]) + u2f(r1[1]));
    auto r2 = __builtin_amdgcn_permlane32_swap(f2u(rr), f2u(rr), false, false);
    const float rp = u2f(r2[0]), rn = u2f(r2[1]);
    hook(IC<5>{});
#pragma unroll
    for (int j = 0; j < 8; ++j) { const f32x2 t = ep[j] * f32x2{rp, rp}; o[j] = pk(t.x, t.y); }
    hook(IC<6>{});
#pragma unroll
    for (int j = 0; j < 8; ++j) { const f32x2 t = en[j] * f32x2{rn, rn}; o[8 + j] = pk(t.x, t.y); }
  };
  auto trbase = [&]() -> unsigned { const unsigned ln = opq(lane), G = ln >> 4; return (8 * (G >> 1) + ((ln & 15) >> 2)) * 64 + (G & 1) * 32 + (ln & 3) * 8; };
  auto dma_tile = [&](int t, int kb) {
    const unsigned g = O_GUT + (opqs(c) * 7 + t) * 28 * 1024 + w * 7168;
    const unsigned l16 = opq(lane) * 16, ld = (unsigned)(kb * 1024 + w * 7168);
    dmak<4>(rs4, g, ld, l16);
    if (t == 3) { dmak<2>(rs4, g + 4096, ld + 4096, l16); if (w < 3) dmak<1>(rs4, g + 4096, ld + 4096, l16, 2); }
    else dmak<3>(rs4, g + 4096, ld + 4096, l16);
  };

  f32x16 ak, av;
  Stats sa;
  {
    const unsigned ln = opq(lane), l16 = ln * 16, G = ln >> 4;
    v4u fr[7], Z[7];
    unsigned ja[7], jd[7], jw;
    bool wcut = false;
    {
      unsigned dst, du1, du2;
      int ct0 = 7 * (jj & 1);
      if (jgu) { const int cc = jq / 7, s = 4 * (jq - 7 * cc) + w; dst = O_GUT + (cc * 196 + s) * 1024; du1 = 28 * 1024; du2 = 56 * 1024; ct0 = 0; }
      else if (jj < 224) { const int ks = jj >> 1, cc = ks / 7, pp = ks - 7 * cc; dst = O_DNT + ((cc >> 2) * 56 + pp * 8 + (cc & 3) * 2) * 1024; du1 = 1024; du2 = 224 * 1024; }
      else { const int hp = (jj - 224) >> 1; dst = O_WOT + hp * 2048; du1 = 1024; du2 = 65536; wcut = hp & 1; }
      if (!has_job) dst = OOB;
      const unsigned jb = jsb + (8 * (G >> 1) + ((ln & 15) >> 2)) * 224 + (ln & 3) * 8;
#pragma unroll
      for (int u = 0; u < 7; ++u) {
        const unsigned e = jgu ? u + 7 * (G & 1) : 2 * u + (G & 1), z = (ln & 31) == (unsigned)u ? ONE2 : 0u;
        const int ct = ct0 + u;
        ja[u] = jb + e * 32 + (e >= 7 ? 3584 - 224 : 0); jd[u] = dst + (ct >> 1) * du2 + (ct & 1) * du1; Z[u] = v4u{z, z, z, z};
        asm volatile("" : "+v"(ja[u]), "+s"(jd[u]), "+v"(Z[u]));
      }
      jw = jsb + ((4 * (ln >> 5)) * 32 + (ln & 31)) * 4;
    }
    auto jstore = [&](int u) { stg(fr[u], rs, l16, jd[u]); };
    unsigned tk[4], xa[4];
    { const unsigned t0 = trbase(), tok = ln & 31, rg = tok >> 3, r8 = tok & 7;
      const unsigned a0 = xsb + rg * 1024 + r8 * 128, sw = (ln >> 5) ^ r8 ^ (rg >> 1) ^ (2 * w);
#pragma unroll
      for (int j = 0; j < 4; ++j) { tk[j] = t0 + (unsigned)(j ^ w) * 2048; xa[j] = a0 + (sw ^ (2 * j)) * 16; asm volatile("" : "+v"(tk[j]), "+v"(xa[j])); } }
    unsigned zj = (unsigned)w * 2048 + (ln & 31) * 64 + 48 + (ln >> 5) * 8192;
    v2u zv = {(ln & 16) ? 0u : 0x3F80u, 0u};
    const v4u PP = P4;
    unsigned skp = S_KVP(0) + w * 2048, sof = S_OFST(0), skr = S_KVRST(w & 1), v64 = lxpat(ln, 64 * 256), vkr = (w >= 2 && ln < 16) ? l16 : OOB;
    unsigned vfs = (ln >> 3) * 32768 + (ln & 7) * 16, sfs = (O_X + (unsigned)q * 128) | (w == 3 ? 0u : 0x40000000u);
    asm volatile("" : "+v"(jw), "+v"(zj), "+v"(zv), "+v"(v64), "+v"(vkr), "+v"(vfs), "+s"(skp), "+s"(sof), "+s"(skr), "+s"(sfs));
    sb();
    chunks(IC<-4>{});
    STAMP(1);
    {
      asm volatile("s_waitcnt vmcnt(%2)" : "+v"(warm), "+v"(warm2) : "n"(walk(E_JW, 0)) : "memory");
      STAMP(32);
#pragma unroll
      for (int u = 0; u < 7; ++u) fr[u] = trf(L + ja[u], 4 * 224);
      sb();
      chunks(IC<-3>{});
      chunks(IC<-2>{});
      if (wcut) {
#pragma unroll
        for (int u = 0; u < 7; ++u) { if (hi) fr[u] = v4u{0u, 0u, fr[u].x, fr[u].y}; asm volatile("" : "+v"(fr[u])); }
      }
      f32x16 jc = {};
#pragma unroll
      for (int u = 0; u < 7; ++u) {
        if (u < NJ) jstore(u);
        jc = mfma(Z[u], fr[u], jc);
      }
      sb();
      { const v4u rp = mkrs(p.wphi, 16 * 784 * 2);
        dmab(rp, l16, (unsigned)(c * 1568), jsb + 4096); dmab(rp, l16, (unsigned)(c * 1568 + 1024), jsb + 5120); }
      chunks(IC<-1>{});
#pragma unroll
      for (int i = 0; i < 4; ++i) *(LAS float*)(L + jw + i * 128) = jc[i];
    }
    STAMP(33);
    JIT_();
    f32x16 cs, gr;
    float t4[4], c4[4], dg; v4u wim;
#define XRD(i, k) xf[4 * (i) + (k)] = *(const LAS v4u*)(L + xa[k] + ((i) % 3) * 4096)
#define KVF(i, m) trf(L + tk[(i) & 3] + ((i) >> 2) * 8192 + (m) * 1024, 256)
    vmwait<walk(E_PL, 0) - mx2(wlast(0, 2), mx2(walk(E_XEND, 0), xrd(1) < 0 ? walk(E_XEND, 1) : 0))>();
    *(LAS v2u*)(L + zj) = zv;
#pragma unroll
    for (int i = 0; i < 8; ++i) if (xrd(i >> 2) < 0) XRD(i >> 2, i & 3);
    bar();
    STAMP(3);
    v4u wb[2][2];
#pragma unroll
    for (int s = 0; s < 2; ++s) { wb[s][0] = KVF(s, 0); wb[s][1] = KVF(s, 1); }
    if (w == 3) {
      const LAS f32x4* sp = (const LAS f32x4*)(L + 84 * 1024 + l16);
      stg(__builtin_bit_cast(v4u, (sp[0] + sp[448]) + (sp[896] + sp[1344])), rs, (jgu && ln < 56) ? l16 : OOB, O_CSG + jq * 896);
    }
    sfor<0, 28>([&](auto N) {
      constexpr int s = decltype(N)::value;
      if constexpr (s == 4) { vmwait<walk(E_STEP, s) - wlast(2, 4)>(); *(LAS v2u*)(L + zj + 16384) = zv; sb(); }
      if constexpr (s == 12) { vmwait<walk(E_STEP, s) - wlast(4, 7)>(); *(LAS v2u*)(L + zj + 32768) = zv; if (ln < 32) *(LAS v2u*)(L + zj + 49152) = zv; sb(); }
      sfor<1, 7>([&](auto I) { constexpr int i = decltype(I)::value; if constexpr (xrd(i) == s) vmwait<walk(E_STEP, s) - walk(E_XEND, i)>(); });
      if constexpr (s == PB) vmwait<walk(E_STEP, s) - walk(E_PEND, 0)>();
      if constexpr (s == 5 || s == 13 || s == 20) JIT_();
      if constexpr (s == 7 || s == 15 || s == PB) rawbar();
      if constexpr (s == PB) stg(v4u{tlo + 1, thi, tlo + 1, thi}, rs, vfs, sfs);
      if constexpr (s == 0) mfv0(ak, wb[s & 1][0], xf[s]); else mfv(ak, wb[s & 1][0], xf[s]);
      if constexpr (s == 0) mfv0(cs, Z[1], wb[s & 1][0]); else if constexpr ((s & 3) == 0) mfv(cs, Z[1], wb[s & 1][0]);
      if constexpr (s + 2 < 28) wb[s & 1][0] = KVF(s + 2, 0);
      if constexpr (s == 0) mfv0(av, xf[s], wb[s & 1][1]); else mfv(av, xf[s], wb[s & 1][1]);
      if constexpr ((s & 3) == 0) mfv(cs, Z[4], wb[s & 1][1]);
      if constexpr (s + 2 < 28) wb[s & 1][1] = KVF(s + 2, 1);
      if constexpr (s == 0) mfv0(gr, xf[s], xf[s]); else if constexpr (s < 24) mfv(gr, xf[s], xf[s]);
      else if constexpr (s < 26) { mfv(gr, xf[2 * s - 24], xf[2 * s - 24]); mfv(gr, xf[2 * s - 23], xf[2 * s - 23]); }
      if constexpr (s == 21) {
        const LAS unsigned char* pb = L + jsb + 4096 + (4 * (G >> 1) + ((ln & 15) >> 2)) * 56 + (G & 1) * 32 + (ln & 3) * 8;
        wph[0] = trf(pb, 8 * 56);
        wph[1] = trf(pb + 16 * 56, 4 * 56);
      }
      if constexpr (s == 20) wim = *(const LAS v4u*)(L + (r + 16 * w) * 1024 + l16);
      if constexpr (s == 24) stg(wim, rs, l16, r + 16 * w < 56 ? O_QKVI + (c * 84 + r + 16 * w) * 1024 : OOB);
      if constexpr (s == 25) { asm volatile("" : "+v"(cs)); *(LAS float*)(L + jsb + 1024 + ln * 4) = hi ? cs[0] : cs[1]; }
      if constexpr (s == 26) {
        asm volatile("" : "+v"(gr));
#pragma unroll
        for (int i = 0; i < 4; ++i) { t4[i] = sel(ln & 16, sel(ln & 8, gr[12 + i], gr[8 + i]), sel(ln & 8, gr[4 + i], gr[i])); PIN(t4[i]); }
        bar();
        const LAS float* sp = (const LAS float*)(L + 84 * 1024 + 1024 + ln * 4);
#pragma unroll
        for (int i = 0; i < 4; ++i) c4[i] = sp[1792 * i];
      }
      if constexpr (s == 27) {
        dg = sel(ln & 2, sel(ln & 1, t4[3], t4[2]), sel(ln & 1, t4[1], t4[0]));
        if (((ln >> 2) & 1) != (ln >> 5)) dg = 0.f;
        dg = hsum(dg) * (1.0f / 448.0f); PIN(dg);
        const float ckv = (c4[0] + c4[1]) + (c4[2] + c4[3]);
        cfa = split2(hi ? ckv : 0.f, hi ? ((ln & 31) == 24 ? 1.0f : 0.0f) : ckv);
        if (!hi) { wph[1].z = 0u; wph[1].w = 0u; }
        asm volatile("" : "+v"(cfa), "+v"(wph[1]));
      }
      sfor<1, 7>([&](auto I) { constexpr int i = decltype(I)::value, d = s - xrd(i); if constexpr (xrd(i) >= 0 && (d == 0 || d == 1)) { XRD(i, 2 * d); XRD(i, 2 * d + 1); } });
      constexpr int k = sat(s);
      if constexpr (k >= 0 && k < 7) jstore(k);
      if constexpr (k == 7 || k == 8) stg(PP, rs, l16 + (k - 7) * 1024, skp);
      if constexpr (k == 9 || k == 10) stg(PP, rs, v64 + (k - 9) * 512, sof);
      if constexpr (k == 11) stg(PP, rs, vkr, skr);
      if constexpr (qcount(s + 1) > qcount(s)) dmab(rqkv, vwq, (unsigned)(4 * qcount(s) + w) * 43008, (unsigned)(56 + 4 * qcount(s) + w) * 1024);
      sfor<0, 7>([&](auto LC) {
        constexpr int lc = decltype(LC)::value;
        if constexpr (lc >= 3 && xpos(lc) == s) asm volatile("" : "+v"(xf[4 * lc - 12]), "+v"(xf[4 * lc - 11]), "+v"(xf[4 * lc - 10]), "+v"(xf[4 * lc - 9]) :: "memory");
        if constexpr (xpos(lc) >= 0 && (s == xpos(lc) || s == xpos(lc) + 1)) xchunk(lc, s - xpos(lc));
        if constexpr (wpos(lc) == s) wchunk(lc);
      });
      sb();
    });
#undef KVF
#undef XRD
    asm volatile("s_nop 1" : "+v"(ak), "+v"(av) :: "memory");
    STAMP(60);
    const float mean = u2f(__builtin_amdgcn_permlane32_swap(f2u(ak[12]), f2u(ak[12]), false, false)[0]) * (1.0f / 448.0f);
    sa = mk_stats(mean, fmaxf(dg - mean * mean, 0.f));
    if (tid < 8) *(LAS unsigned*)(L + L_SIG + tid * 4) = 0u;
  }

#define LDSF(kb) (*(const LAS v4u*)(L + ((kb) < 64 ? lane16 + (kb) * 1024 : (kb) < 128 ? lane16b + ((kb) - 64) * 1024 : lane16c + ((kb) - 128) * 1024)))
#define GEMM2(RD, M0, M1, STATS)                                                                          \
  do {                                                                                                    \
    v4u wb[2][2]; v2u st[7]; unsigned mm = 0;                                                             \
    _Pragma("unroll") for (int s = 0; s < 12; ++s) { XLD(s); sb(); }                                      \
    PRE_;                                                                                                 \
    _Pragma("unroll") for (int s = 0; s < 2; ++s) { wb[s][0] = RD(s, 0); wb[s][1] = RD(s, 1); }           \
    _Pragma("unroll") for (int g = 0; g < 7; ++g) {                                                       \
      if (g < 2)  { _Pragma("unroll") for (int i = 0; i < 4; ++i) mm = mx4(mm, xf[4 * g + i]); }          \
      if (__builtin_expect(__any(mm == POISON), 0)) {     \
                  \
        _Pragma("unroll 1") for (int sp = 0;; ++sp) {                                                     \
          RETRY(VSITE);                                                                                   \
          asm volatile("" ::: "memory");                                                                  \
          _Pragma("unroll") for (int s = 4 * g; s < 28 && s < 12 + 8 * g; ++s) { XLD(s); sb(); }          \
          if (g == 3) { _Pragma("unroll") for (int i = 0; i < 7; ++i) { st[i] = ldg2(rs, vst + i * 512, so2); sb(); } } \
          unsigned n_ = 0;                                                                                \
          _Pragma("unroll") for (int i = 0; i < 4; ++i) n_ = mx4(n_, xf[4 * g + i]);                      \
          if (!__any(n_ == POISON)) break;                                                                \
          if (dead || sp > SPIN_CAP) { dead = 1; break; }                                                 \
          __builtin_amdgcn_s_sleep(1);                                                                    \
        }                                                                                                 \
        if (g > 3) __builtin_amdgcn_s_waitcnt(0x0F70);                                                    \
      }                                                                                                   \
      mm = 0;                                                                                             \
      if (g == 5) {                                                                                       \
        unsigned m_ = 0;                                                                                  \
        _Pragma("unroll") for (int i = 0; i < 7; ++i) m_ = umax(umax(m_, st[i].x), st[i].y);              \
        if (__builtin_expect(__any(m_ == POISON), 0)) {                                                   \
          _Pragma("unroll 1") for (int sp = 0;; ++sp) {                                                   \
            if (dead || sp > SPIN_CAP) { dead = 1; break; }                                               \
            RETRY(52);                                                                                    \
            asm volatile("" ::: "memory");                                                                \
            unsigned n_ = 0;                                                                              \
            _Pragma("unroll") for (int i = 0; i < 7; ++i) st[i] = ldg2(rs, vst + i * 512, so2);           \
            _Pragma("unroll") for (int i = 0; i < 7; ++i) n_ = umax(umax(n_, st[i].x), st[i].y);          \
            if (!__any(n_ == POISON)) break;                                                              \
          }                                                                                               \
        }                                                                                                 \
      }                                                                                                   \
      STAMPG(53 + g);                                                                                     \
      _Pragma("unroll") for (int i = 0; i < 4; ++i) {                                                     \
        const int s = 4 * g + i;                                                                          \
        M0(wb[s & 1][0], xf[s]);                                                                          \
        sb();                                                                                             \
        if (s + 2 < 28) wb[s & 1][0] = RD(s + 2, 0);                                                      \
        if (2 * s + 12 < 28) XLD(2 * s + 12);                                                             \
        if (g >= 1 && g < 6) { mm = mx4(mm, xf[s + 4]); PIN(mm); }                                   \
        HOOK_(s);                                                                                         \
        sb();                                                                                             \
        M1(wb[s & 1][1], xf[s]);                                                                          \
        sb();                                                                                             \
        if (s + 2 < 28) wb[s & 1][1] = RD(s + 2, 1);                                                      \
        if (2 * s + 13 < 28) XLD(2 * s + 13);                                                             \
        if (s >= 8 && s < 12) { st[2 * (s - 8)] = ldg2(rs, vst + 2 * (s - 8) * 512, so2); if (s < 11) st[2 * (s - 8) + 1] = ldg2(rs, vst + (2 * (s - 8) + 1) * 512, so2); } \
        if (s >= 20) ln_step(s - 20, st, STATS);                                                          \
        if (i < 3 || g >= 1) sb();                                                                   \
      }                                                                                                   \
    }                                                                                                     \
  } while (0)

  v4u wf[8][2];
    auto ffn = [&](auto LL) __attribute__((always_inline)) {
      constexpr int l = decltype(LL)::value;
      const unsigned lane16 = opq(lane) * 16;
      const unsigned lane16b = opq(lane16 + 65536), lane16c = opq(lane16 + 131072);
      const unsigned vh = lxpat(opq(lane), 224 * 256);
      const unsigned sh0 = O_HB + l * HBSZ + (unsigned)(56 * w) * 256 + (l == 1 ? WDIS : 0u);
      const unsigned sh[2] = {sh0 + (unsigned)opqs(T32o) * (2 * 224 * 256), sh0 + (unsigned)(opqs(T32o) ^ 1) * (2 * 224 * 256)};
      v4u ha[2][8], hv[3][8], wd5[8][2];
      const unsigned disx = !act ? 0x40000000u : 0u;
      const v4u PP = P4;
      const unsigned ph0 = S_HBST(0), pof = S_OFST(1), px2 = (O_X + 2 * XSZ + S_XST) | disx, ps2 = (O_ST + 2 * STSZ + S_STST) | disx;
      const unsigned v56 = lxpat(opq(lane), 56 * 256), vs8 = lane16 < 512 ? lane16 >> 1 : OOB;
#define PZH(ll, i) do { if (l == 0) stg(PP, rs, vh + (i) * 512, ph0 + (ll) * HBSZ); } while (0)
      STAMP(11 + 16 * l);
      f32x16 ac[7];
      Stats sd;
      {
        const unsigned vx_a = lxpat(opq(lane), 56 * 256), vst = lstpat(opq(lane));
        const unsigned so = S_XA(2 * l + 1), so2 = S_STA(2 * l + 1);
        const unsigned g4 = O_GUT + (opqs(c) * 7 + 4) * 28 * 1024;
#define XLD(s) xf[s] = ldg(rs, vx_a + ((s) & 7) * 512, so + ((s) >> 3) * 4096)
#define RD_(s, t) LDSF(28 * (t) + (s))
#define M0_(wv, xv) do { if (s == 0) mfv0(ac[0], wv, xv); else mfv(ac[0], wv, xv); } while (0)
#define M1_(wv, xv) do { if (s == 0) mfv0(ac[1], wv, xv); else mfv(ac[1], wv, xv); } while (0)
#define VSITE 49
#define PRE_ rawbar()
#define T4_(i) do { if ((i) < 7) dmak<1>(rs4, g4 + w * 7168 + ((i) & 4) * 1024, (unsigned)(112 * 1024 + w * 7168 + ((i) & 4) * 1024), lane16, (i) & 3); \
                     else dmak<1>(rs4, g4 - 1024, (unsigned)((140 + w) * 1024), lane16); } while (0)
#define HOOK_(s) do { if (s == 12 || s == 15 || s == 18) PZH(0, (s - 9) / 3);                                                 \
                      if (l == 1 && s >= 20) T4_(s - 20);                                                                     \
                      if (l == 0 && s == 20) dma_tile(3, 84);                                                                 \
                      if (l == 0 && s >= 24) { T4_(2 * (s - 24)); T4_(2 * (s - 24) + 1); } } while (0)
        GEMM2(RD_, M0_, M1_, sd);
#undef VSITE
#undef PRE_
#undef HOOK_
#undef RD_
#undef M0_
#undef M1_
#undef XLD
      }
      if (l == 1) prog(P_G2 + 16 * l);
      STAMP(41); STAMPC(42);
      FRESH();
      const unsigned shb = S_HBST(l);
      const float nrs = -1.4426950408889634f * sd.rs;
      const unsigned dm2 = pk(-sd.m, -sd.m);
      auto corr = [&](int t) -> v4u {
        const bool on = ((t >> 1) & 1) == hi;
        const unsigned z = on ? dm2 : 0u;
        return (t & 1) ? v4u{0u, z & 0xFFFF0000u, z, 0u} : v4u{z, z & 0xFFFFu, 0u, 0u};
      };
#define FKB(n) ((n) == 55 ? 140 : (n) < 84 ? 56 + (n) : (n) - 84)
      v4u wb[6];
#pragma unroll
      for (int s = 0; s < 4; ++s) wb[s ^ 1] = LDSF(FKB(s ^ 1));
#pragma unroll
      for (int t = 0; t < 2; ++t) mfvn(ac[t], cfd[0], corr(t));
      const unsigned g56 = O_GUT + (opqs(c) * 7 + 5) * 28 * 1024 + w * 14336;
      const unsigned gq = O_QKVI + opqs(c) * 84 * 1024;
      const unsigned sdn = WDIS + O_DNT + (unsigned)(opqs(cg) * 4 + w) * (56 * 1024);
      float tg[4]; unsigned ho[4];
      auto epi_item = [&](auto T, auto K) {
        constexpr int t = decltype(T)::value, k = decltype(K)::value, e0 = k < 8 ? 0 : 4, kk = k & 7;
        if constexpr (k < 16) {
          if constexpr (kk == 0) { _Pragma("unroll") for (int i = 0; i < 4; ++i) { tg[i] = ac[t][e0 + i] * nrs; PIN(tg[i]); } }
          else if constexpr (kk == 1 || kk == 2) { _Pragma("unroll") for (int i = 2 * (kk - 1); i < 2 * kk; ++i) { tg[i] = __builtin_amdgcn_exp2f(tg[i]); PIN(tg[i]); } }
          else if constexpr (kk == 3) { _Pragma("unroll") for (int i = 0; i < 4; ++i) { tg[i] = __builtin_fmaf(tg[i], sd.s, sd.s); PIN(tg[i]); } }
          else if constexpr (kk == 4 || kk == 5) { _Pragma("unroll") for (int i = 2 * (kk - 4); i < 2 * (kk - 3); ++i) { tg[i] = rcp(tg[i]); PIN(tg[i]); } }
          else if constexpr (kk == 6) { _Pragma("unroll") for (int i = 0; i < 4; ++i) { tg[i] *= ac[t][8 + e0 + i]; PIN(tg[i]); } }
          else { ho[e0 >> 1] = san(pk(tg[0], tg[1])); ho[(e0 >> 1) + 1] = san(pk(tg[2], tg[3])); PIN(ho[e0 >> 1]); PIN(ho[(e0 >> 1) + 1]); }
        }
      };
      auto memop = [&](auto U) {
        constexpr int u = decltype(U)::value, k = sched(l, u) / 64, x = sched(l, u) % 64;
        if constexpr (k == K_HS) stg(widen(ho[0], ho[1], ho[2], ho[3]), rs, vh + x * 512, shb);
        if constexpr (k == K_HL) {
          constexpr int g = x >> 3, i = x & 7;
          if constexpr (g == 2 || g == 3 || g == 6) ldv<g * 512>(hv[g == 2 ? 2 : g / 6][i], rs4, vh, sh[i & 1] + (i >> 1) * (14 * 256));
          else lda<g * 512>(ha[g % 4][i], rs4, vh, sh[i & 1] + (i >> 1) * (14 * 256));
        }
        if constexpr (k == K_W5 && x < 6) lda<((x >> 1) & 1) * 2048>(wd5[x >> 1][x & 1], rs4, lane16, sdn + (unsigned)((x & 1) ^ (w & 1)) * 1024 + (x >> 2) * 4096 + 40960);
        if constexpr (k == K_W5 && x >= 6) ldv<((x >> 1) & 1) * 2048>(wd5[x >> 1][x & 1], rs4, lane16, sdn + (unsigned)((x & 1) ^ (w & 1)) * 1024 + (x >> 2) * 4096 + 40960);
        if constexpr (k == K_D56) dmak<2>(rs4, g56 + (x >> 1) * 4096, (unsigned)(w * 14336 + (x >> 1) * 4096), lane16, (x & 1) * 2);
        if constexpr (k == K_DQ) dmak<1>(rs4, gq + (56 + 7 * w + (x & 4)) * 1024, (unsigned)(56 + 7 * w + (x & 4)) * 1024, lane16, x & 3);
        if constexpr (k == K_DK) dmak<1>(rs4, gq + (14 * w + (x & 12)) * 1024, (unsigned)(14 * w + (x & 12)) * 1024, lane16, x & 3);
        if constexpr (k == K_PH) PZH(x >> 3, x & 7);
        if constexpr (k == K_PX) { if constexpr ((x & 3) < 2) stg(PP, rs, v56 + (x & 3) * 512, px2 + (x >> 2) * XSZ); else stg2(v2u{PP.x, PP.y}, rs, vs8, ps2 + (x >> 2) * STSZ); }
        if constexpr (k == K_PO) stg(PP, rs, v56 + ((lane16 & 256) << 3) + x * 512, pof);
      };
      unsigned m0 = 0;
      auto hchk = [&](auto GG, auto JJ, auto UU) {
        constexpr int g = decltype(GG)::value, j = decltype(JJ)::value, u = decltype(UU)::value;
        constexpr int hl[7] = {76, 92, 114, 130, 158, 178, 196};
        constexpr bool inv = g == 2 || g == 3 || g == 6;
        v4u (&hh)[8] = inv ? hv[g == 2 ? 2 : g / 6] : ha[g & 1];
        auto word = [&](int i) -> unsigned { unsigned x; if constexpr (inv) { asm volatile("" : "+v"(hh[i])); x = hh[i].x; } else { asm volatile("" : "+a"(hh[i])); x = hh[i].x; } return x; };
        if constexpr (j == 0) { vmwait<younger(l, g == 5 ? 194 : hl[g] + 14, u)>(); m0 = 0; }
        if constexpr (j < 4) { m0 = umax(umax(m0, word(2 * j)), word(2 * j + 1)); PIN(m0); }
        else if (__builtin_expect(__any(m0 == POISON), 0)) {
#pragma unroll 1
          for (int sp = 0;; ++sp) {
            RETRY(50);
            sfor<hl[g], l == 0 ? hl[g] + 16 : u>([&](auto V) { if constexpr (sched(l, decltype(V)::value) / 64 == K_HL) memop(V); });
            drain();
            unsigned n_ = 0;
#pragma unroll
            for (int i = 0; i < 8; ++i) n_ = umax(n_, word(i));
            if (!__any(n_ == POISON)) break;
            if (dead || sp > SPIN_CAP) { dead = 1; break; }
          }
          if constexpr (l == 0) {
            sfor<hl[g] + 16, u>([&](auto V) { if constexpr (sched(l, decltype(V)::value) / 64 == K_HL) memop(V); });
            drain();
          }
        }
      };
      v2u pf;
      sfor<0, 140>([&](auto N) {
        constexpr int n = decltype(N)::value, t = 2 + n / 28, s = n % 28;
        if constexpr (n % 20 == 0) STAMPD(53 + n / 20);
        if constexpr (s == 0) mfv0(ac[t], cfd[t >> 2], corr(t));
        if constexpr (n == 21 || n == 47 || n == 76) pf = peek();
        if constexpr (n == 23) need(pf, P_G2 + 16 * l);
        if constexpr (n == 18 && l == 0) { vmwait<8 + cum(l, 18)>(); prog(P_G2); }
        if constexpr (n == 35) { vmwait<cum(l, 35)>(); prog(P_T4 + 16 * l); }
        if constexpr (n == 54 && l == 0) vmwait<younger(l, 22, 54)>();
        if constexpr (n == 49) need(pf, P_T4 + 16 * l);
        if constexpr (n == 72) { vmwait<younger(l, 50, 72)>(); prog(P_T56 + 16 * l); }
        if constexpr (n == 78) need(pf, P_T56 + 16 * l);
        mfv(ac[t], wb[n % 6], xf[s]);
        sb();
        if constexpr (n + 4 < 140) wb[((n + 4) ^ 1) % 6] = LDSF(FKB((n + 4) ^ 1));
        constexpr int et = n < 2 ? -1 : n < 20 ? 0 : n < 38 ? 1 : n < 56 ? 2 : n < 58 ? -1 : n < 76 ? 3 : n < 86 ? -1 : n < 104 ? 4 : n < 114 ? -1 : n < 132 ? 5 : -1;
        constexpr int e0 = et == 0 ? 2 : et == 1 ? 20 : et == 2 ? 38 : et == 3 ? 58 : et == 4 ? 86 : 114;
        if constexpr (et >= 0) epi_item(IC<et>{}, IC<n - e0>{});
        if constexpr (n >= 136) hchk(IC<0>{}, IC<n - 136>{}, N);
        memop(N);
        sb();
      });
      prog(P_DD + 16 * l);
      STAMP(12 + 16 * l); STAMPC(43);
      FRESH();
      f32x16 acc[4];
      u64 bad; unsigned lrd;
      sfor<0, 112>([&](auto EE) {
        constexpr int e = decltype(EE)::value, u = 140 + e, g = e >> 4, i = e & 15;
        constexpr int a = g < 6 ? (i & 3) : ((i >> 2) + 1) & 3, k = g < 6 ? i >> 2 : i & 3;
        if constexpr (i == 0) { STAMPE(53 + g); hchk(IC<g>{}, IC<4>{}, IC<u>{}); }
        if constexpr (e == 77) pf = peek();
        if constexpr (e == 79) need(pf, P_DD + 16 * l);
        if constexpr (g == 2 || g == 3) mf_av<false>(acc[a], wdn[4 * g + k][a & 1], hv[g == 2 ? 2 : 0][2 * k + (a >> 1)]);
        else if constexpr (g < 5) mf_aa<(e < 4)>(acc[a], wdn[4 * g + k][a & 1], ha[g % 4][2 * k + (a >> 1)]);
        else if constexpr (g == 5 && k < 3) mf_aa<false>(acc[a], wd5[k][a & 1], ha[1][2 * k + (a >> 1)]);
        else if constexpr (g == 5) mf_va<false>(acc[a], wd5[k][a & 1], ha[1][2 * k + (a >> 1)]);
        else mf_vv<false>(acc[a], wd5[4 + k][a & 1], hv[1][2 * k + (a >> 1)]);
        sb();
        if constexpr (e >= 2 && e < 20) epi_item(IC<6>{}, IC<e - 2>{});
        if constexpr (g < 6 && i >= 12) hchk(IC<g + 1>{}, IC<i - 12>{}, IC<u>{});
        memop(IC<u>{});
        if constexpr (g == 6 && i >= 5 && i < 13) red_put1(acc[(i - 1) >> 2], (i - 1) >> 2, (i - 1) & 3, lane16);
        if constexpr (g == 6 && (i == 13 || i == 14)) { red_put1(acc[3], 3, 2 * (i - 13), lane16); red_put1(acc[3], 3, 2 * (i - 13) + 1, lane16); }
        if constexpr (g == 6 && i == 12) { bad = __builtin_amdgcn_ballot_w64(acc[1][0] != acc[1][0] || acc[2][0] != acc[2][0]); lrd = L_SCR + (w * 3) * 4096 + lane16; asm volatile("" : "+s"(bad), "+v"(lrd)); }
        sb();
      });
      if (__builtin_expect(bad != 0, 0)) {
        asm volatile("s_nop 15" : "+v"(acc[0]) :: "memory");
        RETRY(51);
        f32x16 ar[4] = {};
#pragma unroll 1
        for (int g = 0; g < 7; ++g) {
          v4u hh[8], ww[8];
#define REF_(j) hh[j]
#define REL_(j) hh[j] = ldg(rs, vh + g * 512, sh[(j) & 1] + ((j) >> 1) * (14 * 256))
#pragma unroll
          for (int j = 0; j < 8; ++j) { REL_(j); ww[j] = ldg(rs, lane16 + ((j >> 1) & 1) * 2048, sdn + (unsigned)((j & 1) ^ (w & 1)) * 1024 + (j >> 2) * 4096 + g * 8192); }
          VALIDATES(8, REF_, REL_, 50);
#undef REF_
#undef REL_
#pragma unroll
          for (int j = 0; j < 16; ++j) mfvn(ar[j & 3], ww[2 * (j >> 2) + (j & 1)], hh[2 * (j >> 2) + ((j >> 1) & 1)]);
          asm volatile("s_nop 15" : "+v"(ar[0]), "+v"(ar[1]), "+v"(ar[2]), "+v"(ar[3]));
        }
        asm volatile("s_nop 15\n\ts_nop 7" ::: "memory");
#pragma unroll
        for (int j = 1; j < 4; ++j) red_put(ar[j], j, lane16);
        acc[0] = ar[0];
      }
      STAMP(13 + 16 * l); STAMPC(44);
      FRESH();
      finish(acc[0], 2, l == 1, lrd);
      STAMP(14 + 16 * l);
    };
  auto attn = [&](auto LL) __attribute__((always_inline)) {
    constexpr int l = decltype(LL)::value;
    const unsigned lane16 = opq(lane) * 16;
    STAMP(4 + 16 * l);
    FRESH();
    const unsigned nm2 = pk(-sa.m, -sa.m);
#define WD1(i) do { if (l == 0) wdn[(i) >> 1][(i) & 1] = ldg(rs, lane16 + (((i) >> 1) & 1) * 2048, WDIS + O_DNT + (unsigned)(cg * 4 + w) * (56 * 1024) + (unsigned)(((i) & 1) ^ (w & 1)) * 1024 + ((i) >> 2) * 4096); } while (0)
    {
      LAS unsigned char* const SW = L + L_SCA + w * SCA_ST;
      v2u fl;
      const unsigned sfl = O_X + (unsigned)((opqs(b) & 7) * 4 + w) * 8192, vfl = lane16 * 8;
      if (l == 0) { fl = ldg2(rs, vfl, sfl); sb(); }
      mfvn(ak, cfa, hi ? v4u{0u, 0u, 0u, 0u} : v4u{0u, nm2 & 0xFFFF0000u, nm2, 0u});
      mfvn(av, hi ? v4u{nm2, pk(-sa.m, sa.s), 0u, 0u} : v4u{0u, 0u, 0u, 0u}, cfa);
      asm volatile("s_nop 4" : "+v"(ak));
      f32x16 pkk;
      mfv0(pkk, wph[0], v4u{pk(ak[0], ak[1]), pk(ak[2], ak[3]), pk(ak[4], ak[5]), pk(ak[6], ak[7])});
      mfvn(pkk, wph[1], v4u{pk(ak[8], ak[9]), pk(ak[10], ak[11]), pk(ak[12], ak[13]), pk(ak[14], ak[15])});
      asm volatile("" : "+v"(av));
      *(LAS v4u*)(SW + 4352 + lane16) = v4u{pk(av[0], av[1]), pk(av[2], av[3]), pk(av[4], av[5]), pk(av[6], av[7])};
      *(LAS v4u*)(SW + 5376 + lane16) = v4u{pk(av[8], av[9]), pk(av[10], av[11]), pk(av[12], av[13]), pk(av[14], av[15])};
      const unsigned ln2 = opq(lane), G2 = ln2 >> 4;
      unsigned phw = L_SCA + w * SCA_ST + (ln2 & 31) * 136 + (ln2 >> 5) * 8;
      unsigned tba = L_SCA + (4 * (G2 >> 1) + ((ln2 & 15) >> 2)) * 136 + (16 * (G2 & 1) + 4 * (ln2 & 3)) * 2 + w * 64, tbb = L_SCA + 4352 + lane16;
      v4u PK; unsigned skp1;
      if (l == 0) { PK = P4; skp1 = S_KVP(1) + w * 2048; asm volatile("" : "+s"(skp1)); }
      asm volatile("s_nop 1" : "+v"(pkk), "+v"(phw), "+v"(tba), "+v"(tbb) :: "memory");
      unsigned ph[16];
      dual_softmax(pkk, sa.rs, sa.rs, ph, [&](auto H) {
        constexpr int h = decltype(H)::value;
        if constexpr (l == 0 && (h == 0 || h == 2)) { sb(); stg(PK, rs, lane16 + h * 512, skp1); sb(); }
      });
#pragma unroll
      for (int t = 0; t < 2; ++t)
#pragma unroll
        for (int qq = 0; qq < 4; ++qq) *(LAS v2u*)(L + phw + t * 64 + qq * 16) = v2u{ph[8 * t + 2 * qq], ph[8 * t + 2 * qq + 1]};
      if (l == 0) {
        drain();
        asm volatile("" : "+v"(fl));
#pragma unroll 1
        for (int sp = 0; !__all(fl.y == thi && fl.x - tlo - 1u < 8u); ++sp) {
          if (dead || sp > SPIN_CAP) { dead = 1; break; }
          __builtin_amdgcn_s_sleep(2);
          asm volatile("" ::: "memory");
          fl = ldg2(rs, vfl, sfl);
          asm volatile("" : "+v"(fl));
        }
      }
      bar();
      STAMP(34 + l);
      if (w < 2) {
        v4u fa[8], fb[8];
#pragma unroll
        for (int i = 0; i < 8; ++i) {
          const int ww = i >> 1, s = i & 1;
          fa[i] = trf(L + tba + ww * SCA_ST + (16 * s) * 136, 8 * 136);
          fb[i] = *(const LAS v4u*)(L + tbb + ww * SCA_ST + s * 1024);
        }
        unsigned so = S_KVP(l) + w * 4096;
        asm volatile("" : "+s"(so));
        sb();
        f32x16 kv;
#pragma unroll
        for (int i = 0; i < 8; ++i) { if (i == 0) mfv0(kv, fa[i], fb[i]); else mfv(kv, fa[i], fb[i]); }
        asm volatile("s_nop 11" : "+v"(kv));
#pragma unroll
        for (int qq = 0; qq < 4; ++qq)
          stg(v4u{san(f2u(kv[4 * qq])), san(f2u(kv[4 * qq + 1])), san(f2u(kv[4 * qq + 2])), san(f2u(kv[4 * qq + 3]))}, rs, lane16 + qq * 1024, so);
      }
    }
    STAMP(5 + 16 * l);
    FRESH();
    if (l == 0 && w == 2) {
#pragma unroll
      for (int i = 0; i < 2; ++i) { const int g = r + 16 * i; stg(*(const LAS v4u*)(L + (56 + g) * 1024 + lane16), rs, lane16, g < 28 ? O_QKVI + (c * 84 + 56 + g) * 1024 : OOB); }
    }
    if (l == 1 && w == 3) {
      const unsigned g = O_GUT + (opqs(c) * 7) * 28 * 1024;
#pragma unroll 1
      for (int i = 0; i < 14; ++i) dmak<4>(rs4, g + i * 4096, (unsigned)i * 4096, lane16);
    }
    const unsigned g01 = O_GUT + (opqs(c) * 7) * 28 * 1024 + w * 14336;
    STAMP(36 + l);
    f32x16 aq;
    v4u pa[8];
    constexpr int ESL = l == 0 ? 26 : -1;
    v4u PQ; unsigned pvx, pvh, pvs, psx, psh, pss;
    auto pois = [&](int i) {
      if (i < 2) stg(PQ, rs, pvx + i * 512, psx); else if (i == 2) stg2(v2u{PQ.x, PQ.y}, rs, pvs, pss); else stg(PQ, rs, pvh + (i - 3) * 512, psh);
    };
    {
      unsigned tb[4];
      { const unsigned t0 = trbase() + 56 * 1024;
#pragma unroll
        for (int j = 0; j < 4; ++j) tb[j] = t0 + (unsigned)(j ^ w) * 1024; }
#define QF(i) trf(L + tb[(i) & 3] + ((i) >> 2) * 4096, 256)
      v4u wb[4];
#pragma unroll
      for (int s = 0; s < 4; ++s) wb[s] = QF(s);
      sb();
      PQ = P4; pvx = lxpat(lane16 >> 4, 56 * 256); pvh = lxpat(lane16 >> 4, 224 * 256); pvs = lane16 < 512 ? lane16 >> 1 : OOB;
      psx = (O_X + (2 * l + 1) * XSZ + S_XST) | WDIS; pss = (O_ST + (2 * l + 1) * STSZ + S_STST) | WDIS; psh = S_HBST(l);
      if (l == 1 && w == 2) { psx |= 0x20000000u; pss |= 0x20000000u; psh |= 0x20000000u; }
      asm volatile("" : "+v"(pvx), "+v"(pvh), "+v"(pvs), "+s"(psx), "+s"(pss), "+s"(psh));
      f32x16 cs;
      const unsigned z0 = (lane16 & 511) == 0 ? ONE2 : 0u;
      v4u Z0 = {z0, z0, z0, z0};
      if (l == 0) asm volatile("" : "+v"(Z0));
      if (l == 1) mfv0(aq, cfa, hi ? v4u{0u, 0u, 0u, 0u} : v4u{nm2, nm2 & 0xFFFFu, 0u, 0u});
      sfor<0, 28>([&](auto N) {
        constexpr int s = decltype(N)::value;
        if constexpr (s == 0 && l == 0) mfv0(aq, wb[s & 3], xf[s]); else mfv(aq, wb[s & 3], xf[s]);
        if constexpr (l == 0 && s == 0) mfv0(cs, Z0, wb[s & 3]); else if constexpr (l == 0 && (s & 3) == 0) mfv(cs, Z0, wb[s & 3]);
        if constexpr (s + 4 < 28) wb[s & 3] = QF(s + 4);
        if constexpr (s == ESL) {
          if (w == 2) {
            const unsigned vo = (lane16 >> 8) * 8192 + (lane16 & 255), so = O_KVP + l * KVPSZ + (unsigned)(c * 128 + 2 * (r >> 2)) * 1024 + (unsigned)(r & 3) * 256;
#pragma unroll
            for (int i = 0; i < 8; ++i) pa[i] = ldg(rs, vo, so + (i & 1) * 1024 + (i >> 1) * 32768);
          } else {
#pragma unroll
            for (int i = 0; i < 8; ++i) asm volatile("" : "=v"(pa[i]));
          }
        }
        if constexpr (l == 1 && s % 3 == 0) pois(s / 3);
        if constexpr (l == 0 && s % 8 == 2) pois(s / 8);
        if constexpr (l == 0 && (s & 1)) dmak<1>(rs4, g01 + (s >> 3) * 4096, (unsigned)(w * 14336 + (s >> 3) * 4096), lane16, (s >> 1) & 3);
        sb();
      });
#undef QF
      asm volatile("s_nop 11" : "+v"(aq) :: "memory");
      if (l == 0) { *(LAS float*)(L + 84 * 1024 + 7168 * w + 2048 + (lane16 >> 2)) = cs[0]; prog(P_J); }
    }
    unsigned pq[16];
    {
      v4u rv[2];
      STAMP(6 + 16 * l);
      FRESH();
      const int fr = r >> 2, lq = r & 3;
      const unsigned ln = opq(lane), vo = (ln >> 4) * 8192 + (ln & 15) * 16;
      const unsigned so = O_KVP + l * KVPSZ + (unsigned)(c * 128 + 2 * fr) * 1024 + (unsigned)lq * 256;
#define REF_(i) pa[i]
#define REL_(i) pa[i] = ldg(rs, vo, so + ((i) & 1) * 1024 + ((i) >> 1) * 32768)
      if (ESL >= 0) {
      } else if (w == 2) {
#pragma unroll
        for (int i = 0; i < 8; ++i) REL_(i);
      } else {
#pragma unroll
        for (int i = 0; i < 8; ++i) asm volatile("" : "=v"(pa[i]));
      }
      v4u c0, c1;
      if (l == 0) {
        const unsigned v5 = ln < 56 ? ln * 16 : OOB, s5 = O_CSG + (unsigned)(c * 7 + 2 * w) * 896;
        c0 = ldg(rs, v5, s5); c1 = ldg(rs, v5, w < 3 ? s5 + 896 : 0x40000000u);
        const GAS v4u* xp = (const GAS v4u*)(p.x + (size_t)(32 * T32o + (ln & 31)) * 448 + 64 * cg + 32 * (w & 1) + 8 * (ln >> 5));
        rv[0] = xp[0]; rv[1] = xp[2];
      }
      auto reduce = [&]() { if (w == 2) {
        unsigned vr = (ln & 15) * 16 + (ln >> 4) * 4, sr = S_KVRST(l);
        asm volatile("" : "+v"(vr), "+s"(sr));
        auto sum = [&]() -> f32x2 {
          f32x4 t[2]; float u[4];
#pragma unroll
          for (int z = 0; z < 2; ++z) t[z] = (__builtin_bit_cast(f32x4, pa[z]) + __builtin_bit_cast(f32x4, pa[2 + z])) + (__builtin_bit_cast(f32x4, pa[4 + z]) + __builtin_bit_cast(f32x4, pa[6 + z]));
#pragma unroll
          for (int i = 0; i < 4; ++i) { auto x = __builtin_amdgcn_permlane32_swap(f2u(t[0][i]), f2u(t[1][i]), false, false); u[i] = u2f(x[0]) + u2f(x[1]); }
          auto y0 = __builtin_amdgcn_permlane16_swap(f2u(u[0]), f2u(u[2]), false, false), y1 = __builtin_amdgcn_permlane16_swap(f2u(u[1]), f2u(u[3]), false, false);
          return f32x2{u2f(y0[0]) + u2f(y0[1]), u2f(y1[0]) + u2f(y1[1])};
        };
        f32x2 sv = sum();
        if (__builtin_expect(__any(sv.x != sv.x || sv.y != sv.y), 0)) {
          REFETCH(8, REF_, REL_, 46);
#pragma unroll
          for (int i = 0; i < 8; ++i) asm volatile("" : "+v"(pa[i]));
          sv = sum();
        }
#undef REF_
#undef REL_
        __builtin_amdgcn_raw_buffer_store_b32(san(pk(sv.x, sv.y)), rs, vr, sr, 16);
        if (l == 1) {
          psx &= ~0x20000000u; pss &= ~0x20000000u; psh &= ~0x20000000u;
#pragma unroll
          for (int i = 0; i < 10; ++i) pois(i);
        }
      } };
      if (l == 1) reduce();
      STAMP(15 + 16 * l);
      if (l == 0) {
        await(P_J);
        const LAS float* sp = (const LAS float*)(L + 84 * 1024 + 1024 + (ln & 31) * 4);
        const float ck = (sp[0] + sp[1792]) + (sp[3584] + sp[5376]), cq = (sp[256] + sp[2048]) + (sp[3840] + sp[5632]);
        if (!hi) cfa = split2(cq, ck);
      }
      if (l == 0) {
        mfvn(aq, cfa, hi ? v4u{0u, 0u, 0u, 0u} : v4u{nm2, nm2 & 0xFFFFu, 0u, 0u});
        asm volatile("s_nop 12" : "+v"(aq));
      }
      f32x16 pp;
      mfv0(pp, wph[0], v4u{pk(aq[0], aq[1]), pk(aq[2], aq[3]), pk(aq[4], aq[5]), pk(aq[6], aq[7])});
      mfvn(pp, wph[1], v4u{pk(aq[8], aq[9]), pk(aq[10], aq[11]), pk(aq[12], aq[13]), pk(aq[14], aq[15])});
      if (l == 0) reduce();
      asm volatile("s_nop 11" : "+v"(pp));
      const unsigned swo = WDIS + O_WOT + (unsigned)(opqs(cg) * 4 + w) * 16384;
      dual_softmax(pp, sa.rs, 1.0f, pq, [&](auto H) {
        constexpr int h = decltype(H)::value;
        if constexpr (l == 0) {
          sb();
#pragma unroll
          for (int j = (h * 16) / 7; j < ((h + 1) * 16) / 7; ++j)
            wf[j >> 1][j & 1] = ldg(rs, lane16 + ((j >> 1) & 1) * 2048, swo + (unsigned)((j & 1) ^ (w & 1)) * 1024 + (j >> 2) * 4096);
          sb();
        }
      });
#pragma unroll
      for (int i = 0; i < 16; ++i) asm volatile("" : "+v"(pq[i]));
      if (l == 0) {
        *(LAS f32x4*)(L + 84 * 1024 + 7168 * w + 3072 + ln * 16) = __builtin_bit_cast(f32x4, c0) + __builtin_bit_cast(f32x4, c1);
#pragma unroll
        for (int i = 0; i < 2; ++i) { const v4u t = widen(rv[i].x, rv[i].y, rv[i].z, rv[i].w); rsd[4 * i] = t.x; rsd[4 * i + 1] = t.y; rsd[4 * i + 2] = t.z; rsd[4 * i + 3] = t.w; }
      }
      STAMP(38 + l);
      vmwait<l == 0 ? 16 : 0>();
    }
    bar();
    STAMP(7 + 16 * l);
    FRESH();
    if (l == 1) __builtin_amdgcn_s_sleep(8);
    {
      v4u kf[4];
      const unsigned so = O_KVR + l * KVRSZ + (unsigned)opqs(c) * 4096;
#define REF_(i) kf[i]
#define REL_(i) kf[i] = ldg(rs, lane16 + (i) * 1024, so)
#pragma unroll
      for (int i = 0; i < 4; ++i) REL_(i);
      unsigned v64 = lxpat(opq(lane), 64 * 256), s2 = S_OFST(l);
      asm volatile("" : "+v"(v64), "+s"(s2));
      sb();
      f32x16 nm;
      unsigned mk = 0;
#pragma unroll
      for (int i = 0; i < 4; ++i) {
        if (i == 0) mf_vv<true>(nm, kf[i], v4u{pq[4 * i], pq[4 * i + 1], pq[4 * i + 2], pq[4 * i + 3]}); else mf_vv<false>(nm, kf[i], v4u{pq[4 * i], pq[4 * i + 1], pq[4 * i + 2], pq[4 * i + 3]});
        mk = mx4(mk, kf[i]); PIN(mk);
      }
      if (__builtin_expect(__any(mk == POISON), 0)) {
        asm volatile("s_nop 11" : "+v"(nm) :: "memory");
        REFETCH(4, REF_, REL_, 47);
#pragma unroll
        for (int i = 0; i < 4; ++i) { if (i == 0) mfv0(nm, kf[i], v4u{pq[4 * i], pq[4 * i + 1], pq[4 * i + 2], pq[4 * i + 3]}); else mfv(nm, kf[i], v4u{pq[4 * i], pq[4 * i + 1], pq[4 * i + 2], pq[4 * i + 3]}); }
        asm volatile("s_nop 11" : "+v"(nm));
      }
#undef REF_
#undef REL_
      asm volatile("s_nop 7" : "+v"(nm));
      auto rd = __builtin_amdgcn_permlane32_swap(f2u(nm[12]), f2u(nm[12]), false, false);
      const float den = bfr(bfr(u2f(rd[0])) + 1e-6f);
      const float rden = rcp(den);
      unsigned o[8];
#pragma unroll
      for (int i = 0; i < 8; ++i) o[i] = san(pk(nm[2 * i] * rden, nm[2 * i + 1] * rden));
      if (!hi) { o[6] = 0u; o[7] = 0u; }
      stg(widen(o[0], o[1], o[2], o[3]), rs, v64, s2);
      stg(widen(o[4], o[5], o[6], o[7]), rs, v64 + 512, s2);
    }
    STAMP(8 + 16 * l);
    FRESH();
    unsigned cw = touch();
    dma_tile(2, 56);
    sb();
#pragma unroll
    for (int i = 22; i < 33; ++i) WD1(i);
    sb();
    if (l == 0) {
      const LAS float* sp = (const LAS float*)(L + opq(84 * 1024 + 3072 + ((lane16 >> 2) & 127) + (lane16 & 512) / 2));
      float sm[4];
#pragma unroll
      for (int i = 0; i < 4; ++i) { const int o_ = (4 * (i >> 1) + (i & 1)) * 32; sm[i] = (sp[o_] + sp[o_ + 1792]) + (sp[o_ + 3584] + sp[o_ + 5376]); }
      cfd[0] = split2(sm[0], sm[1]); cfd[1] = split2(sm[2], hi ? 0.0f : sm[3]);
      asm volatile("" : "+v"(cfd[0]), "+v"(cfd[1]));
    }
    vmwait<l == 0 ? 11 : 0>();
    asm volatile("" : "+v"(cw));
    __builtin_amdgcn_s_sleep(4);
    {
      v4u of[8][2];
      const unsigned vo = lxpat(opq(lane), 64 * 256);
      const unsigned so0 = O_OF + l * OFSZ + (unsigned)(16 * w) * 256;
      const unsigned so[2] = {so0 + (unsigned)opqs(T32o) * (2 * 64 * 256), so0 + (unsigned)(opqs(T32o) ^ 1) * (2 * 64 * 256)};
#define KS_(i) (2 * (((i) >> 1) & 3) + ((i) < 8 ? 1 : 0))
#define OLD(i) of[KS_(i)][(i) & 1] = ldg(rs, vo + KS_(i) * 512, so[(i) & 1])
#pragma unroll
      for (int i = 0; i < 16; ++i) { OLD(i); sb(); }
      f32x16 acc[4];
      unsigned mm = 0, lrd;
#pragma unroll
      for (int i = 0; i < 8; ++i) mm = mx4(mm, of[KS_(i)][i & 1]);
#define VSITE 48
      if (__builtin_expect(__any(mm == POISON), 0)) {
#pragma unroll 1
        for (int sp = 0;; ++sp) {
          RETRY(VSITE);
          asm volatile("" ::: "memory");
#pragma unroll
          for (int i = 0; i < 16; ++i) { OLD(i); sb(); }
          unsigned n_ = 0;
#pragma unroll
          for (int i = 0; i < 8; ++i) n_ = mx4(n_, of[KS_(i)][i & 1]);
          if (!__any(n_ == POISON)) break;
          if (dead || sp > SPIN_CAP) { dead = 1; break; }
          __builtin_amdgcn_s_sleep(1);
        }
      }
      mm = 0;
#pragma unroll
      for (int i = 0; i < 16; ++i) {
        const int a = i & 3, ks = 2 * (i >> 2) + 1;
        if (i < 4) mf_av<true>(acc[a], wf[ks][a & 1], of[ks][a >> 1]); else mf_av<false>(acc[a], wf[ks][a & 1], of[ks][a >> 1]);
        if (i >= 8) { mm = mx4(mm, of[KS_(i)][i & 1]); PIN(mm); }
        if (i < 14) WD1(i);
        sb();
      }
      if (__builtin_expect(__any(mm == POISON), 0)) {
#define REF_(i) of[KS_(8 + (i))][(i) & 1]
#define REL_(i) OLD(8 + (i))
        REFETCH(8, REF_, REL_, 48);
#undef REF_
#undef REL_
      }
#undef VSITE
#pragma unroll
      for (int i = 0; i < 16; ++i) {
        const int a = ((i >> 2) + 1) & 3, k2 = i & 3;
        mf_av<false>(acc[a], wf[2 * k2][a & 1], of[2 * k2][a >> 1]);
        if (i & 1) WD1(14 + (i >> 1));
        if (i >= 5 && i < 13) red_put1(acc[(i - 1) >> 2], (i - 1) >> 2, (i - 1) & 3, lane16);
        if (i == 13 || i == 14) { red_put1(acc[3], 3, 2 * (i - 13), lane16); red_put1(acc[3], 3, 2 * (i - 13) + 1, lane16); }
        if (i == 2) { lrd = L_SCR + (w * 3) * 4096 + lane16; PIN(lrd); }
        sb();
      }
#undef KS_
#undef OLD
      STAMP(9 + 16 * l);
      FRESH();
      finish(acc[0], 2 * l + 1, false, lrd);
    }
    STAMP(10 + 16 * l);
    FRESH();
    sb();
#pragma unroll
    for (int i = 33; i < 40; ++i) WD1(i);
    sb();
    vmwait<l == 0 ? 8 : 1>();
    if (!act) drain();
    __builtin_amdgcn_s_sleep(SLD);
  };
  attn(IC<0>{});
  ffn(IC<0>{});
  {
    {
      const unsigned lane16 = opq(lane) * 16;
      unsigned cw = touch();
      vmwait<2>();
      if (!act) drain();
      __builtin_amdgcn_s_sleep(SLA);
      STAMP(19);
      FRESH();
      const unsigned vx_a = lxpat(opq(lane), 56 * 256), vst = lstpat(opq(lane));
      const unsigned so = S_XA(2), so2 = S_STA(2);
      unsigned tb[4], vx[4];
      { const unsigned t0 = trbase();
#pragma unroll
        for (int j = 0; j < 4; ++j) { tb[j] = t0 + (unsigned)(j ^ w) * 2048; vx[j] = vx_a + (unsigned)(j ^ w) * 512; } }
#define XLD(s) xf[s] = ldg(rs, vx[(s) & 3] + ((s) & 4) * 512, so + ((s) >> 3) * 4096)
#define RD_(i, m) trf(L + tb[(i) & 3] + ((i) >> 2) * 8192 + (m) * 1024, 256)
#define M0_(wv, xv) do { if (s == 0) mfv0(ak, wv, xv); else mfv(ak, wv, xv); } while (0)
#define M1_(wv, xv) do { if (s == 0) mfv0(av, xv, wv); else mfv(av, xv, wv); } while (0)
#define VSITE 45
#define PRE_ rawbar()
      const unsigned swo = WDIS + O_WOT + (unsigned)(opqs(cg) * 4 + w) * 16384;
#define HOOK_(s) do { if (s >= 12) wf[(s - 12) >> 1][s & 1] = ldg(rs, lane16 + (((s - 12) >> 1) & 1) * 2048, swo + (unsigned)((s & 1) ^ (w & 1)) * 1024 + ((s - 12) >> 2) * 4096); } while (0)
      GEMM2(RD_, M0_, M1_, sa);
      asm volatile("" : "+v"(cw));
#undef VSITE
#undef PRE_
#undef HOOK_
#undef RD_
#undef M0_
#undef M1_
#undef XLD
    }
  }
  attn(IC<1>{});
  ffn(IC<1>{});
  if (b == 0 && tid == 0) *(GAS u64*)(p.ws + O_CTRL) = ((((u64)thi << 32) | tlo) >> 8 ^ 0x1E3779B97F4A7C15ull) + 1ull;
  asm volatile(".subsection 2\n.Lmega_code_end:\n\t.fill 128, 4, 0xBF800000\n.subsection 0\n");
  STAMP(61); STAMPC(63);
}

}

extern "C" void kernel_launch(void* const* d_in, const int* in_sizes, int n_in, void* d_out, int out_size, void* d_ws,
                              size_t ws_size, hipStream_t stream) {
  Params p;
  p.x = (const unsigned short*)d_in[0];
  p.wqkv = (const unsigned short*)d_in[1];
  p.wphi = (const unsigned short*)d_in[2];
  p.wo = (const unsigned short*)d_in[3];
  p.wg = (const unsigned short*)d_in[5];
  p.wu = (const unsigned short*)d_in[6];
  p.wd = (const unsigned short*)d_in[7];
  p.out = (unsigned short*)d_out;
  p.ws = (unsigned char*)d_ws;
  (void)hipFuncSetAttribute((const void*)mega, hipFuncAttributeMaxDynamicSharedMemorySize, LDS_BYTES);
  hipLaunchKernelGGL(mega, dim3(256), dim3(256), LDS_BYTES, stream, p);
}
```
